```python
import torch

def _softmax(x, axis=-1):
    return torch.softmax(x, dim=axis)

def _gelu(x):
    return torch.nn.functional.gelu(x, approximate='tanh')

def _silu(x):
    return torch.nn.functional.silu(x)

def _sigmoid(x):
    return torch.sigmoid(x)

def _layernorm(x, axis=-1, eps=1e-05):
    m = x.mean(dim=axis, keepdim=True)
    v = x.var(dim=axis, keepdim=True, unbiased=False)
    return (x - m) / torch.sqrt(v + eps)

def _scatter_add(tgt, idx, src, axis=0):
    return tgt.clone().index_add_(axis, idx, src)

def _segment_sum(src, seg, n):
    out = torch.zeros((n,) + src.shape[1:], dtype=src.dtype)
    return out.index_add_(0, seg, src)

def _pad(x, pw):
    flat = tuple((v for pair in reversed(pw) for v in pair))
    return torch.nn.functional.pad(x, flat)
kernel_name = 'adaLN_cond__sliding_local_dilated__glu_gated_swiglu__prompt'

def setup_inputs():

    _w = lambda a: (lambda t: t.to(torch.bfloat16) if t.is_floating_point() else t)(torch.as_tensor(a))

    def _A_setup():
        B, S, D = (1, 2048, 512)
        return {'x': _w((lambda *s: torch.randn(*s, dtype=torch.bfloat16) * 0.05)(B, S, D)), 'wqkv': _w((lambda *s: torch.randn(*s, dtype=torch.bfloat16) * 0.1)(D, 3 * D)), 'wo': _w((lambda *s: torch.randn(*s, dtype=torch.bfloat16) * 0.1)(D, D))}
    _A_in = _A_setup()

    def _B_setup():
        B, S, D, F = (1, 2048, 512, 1376)
        return {'x': _w((lambda *s: torch.randn(*s, dtype=torch.bfloat16) * 0.05)(B, S, D)), 'w1': _w((lambda *s: torch.randn(*s, dtype=torch.bfloat16) * 1.6)(D, F) * (2.0 / D) ** 0.5), 'w3': _w((lambda *s: torch.randn(*s, dtype=torch.bfloat16) * 1.6)(D, F) * (2.0 / D) ** 0.5), 'w2': _w((lambda *s: torch.randn(*s, dtype=torch.bfloat16) * 1.6)(F, D) * (2.0 / F) ** 0.5)}
    _B_in = _B_setup()
    return {'A_x': _A_in['x'], 'A_wqkv': _A_in['wqkv'], 'A_wo': _A_in['wo'], 'B_x': _B_in['x'], 'B_w1': _B_in['w1'], 'B_w3': _B_in['w3'], 'B_w2': _B_in['w2'], 'c': _w((lambda *s: torch.randn(*s, dtype=torch.bfloat16) * 0.2)(1, 512) * 8.0), 'w_mod': _w((lambda *s: torch.randn(*s, dtype=torch.bfloat16) * 0.2)(512, 6 * 512) * 512 ** (-0.5) * 8.0)}

def reference(A_x, A_wqkv, A_wo, B_x, B_w1, B_w3, B_w2, c, w_mod):

    def _A_ref(x, wqkv, wo):
        B, S, D = x.shape
        H = 32
        Dh = D // H
        W, dil = (8, 2)
        qkv = (lambda x, *d: x.permute(*d))((x @ wqkv).reshape(B, S, 3, H, Dh), 2, 0, 3, 1, 4)
        q, k, v = (qkv[0], qkv[1], qkv[2])
        scale = Dh ** (-0.5)
        pos = torch.arange(S)
        off = torch.arange(W) * dil
        idx = pos[:, None] - off[None, :]
        mask = idx >= 0
        idx_c = torch.where(mask, idx, 0).reshape(-1)
        kg = k[:, :, idx_c, :].reshape(B, H, S, W, Dh)
        vg = v[:, :, idx_c, :].reshape(B, H, S, W, Dh)
        logits = torch.einsum('bhsd,bhswd->bhsw', q, kg) * scale
        logits = torch.where(mask[None, None, :, :], logits, -1000000000.0)
        attn = _softmax(logits, axis=-1)
        o = torch.einsum('bhsw,bhswd->bhsd', attn, vg)
        return (lambda x, *d: x.permute(*d))(o, 0, 2, 1, 3).reshape(B, S, D) @ wo

    def _B_ref(x, w1, w3, w2):
        gate = torch.einsum('bsd,df->bsf', x, w1)
        up = torch.einsum('bsd,df->bsf', x, w3)
        h = _silu(gate) * up
        out = torch.einsum('bsf,fd->bsd', h, w2)
        return out
    x = A_x
    mod = (_silu(c) @ w_mod).reshape(c.shape[0], 6, 1, -1)
    sa, ca, ga, sb, cb, gb = (mod[:, 0].clone(), mod[:, 1].clone(), mod[:, 2].clone(), mod[:, 3].clone(), mod[:, 4].clone(), mod[:, 5].clone())
    h = _layernorm(x, axis=-1) * (1 + ca) + sa
    x = x + ga * _A_ref(h, A_wqkv, A_wo)
    h = _layernorm(x, axis=-1) * (1 + cb) + sb
    x = x + gb * _B_ref(h, B_w1, B_w3, B_w2)
    return x
```

```cpp
#include <hip/hip_runtime.h>
#include <hip/hip_bf16.h>
#include <stdint.h>

typedef unsigned short u16;
typedef __attribute__((ext_vector_type(8))) short bf16x8;
typedef __attribute__((ext_vector_type(4))) float f32x4;

__device__ __forceinline__ float bf2f(u16 b) { return __builtin_bit_cast(float, (unsigned)b << 16); }
__device__ __forceinline__ u16 f2bf(float f) { __hip_bfloat16 h = __float2bfloat16(f); return __builtin_bit_cast(u16, h); }
__device__ __forceinline__ float rbf(float f) { return bf2f(f2bf(f)); }

constexpr int S = 2048, D = 512, F = 1376, H = 32, DH = 16;

__global__ void k_mod(const u16* c, const u16* wmod, float* mod) {
  int n = blockIdx.x * blockDim.x + threadIdx.x;
  float acc = 0.f;
  for (int k = 0; k < D; ++k) {
    float cv = bf2f(c[k]);
    float s = rbf(cv / (1.f + __expf(-cv)));
    acc += s * bf2f(wmod[(size_t)k * 3072 + n]);
  }
  mod[n] = rbf(acc);
}

template <bool F32IN>
__global__ void k_ln(const void* xin, const float* shift, const float* scale, u16* h) {
  int row = blockIdx.x;
  int t = threadIdx.x;
  float v[8];
  for (int j = 0; j < 8; ++j) {
    int col = t * 8 + j;
    v[j] = F32IN ? ((const float*)xin)[(size_t)row * D + col] : bf2f(((const u16*)xin)[(size_t)row * D + col]);
  }
  float s = 0.f;
  for (int j = 0; j < 8; ++j) s += v[j];
  for (int o = 32; o; o >>= 1) s += __shfl_xor(s, o);
  float m = s / D;
  float q = 0.f;
  for (int j = 0; j < 8; ++j) q += (v[j] - m) * (v[j] - m);
  for (int o = 32; o; o >>= 1) q += __shfl_xor(q, o);
  float r = rsqrtf(q / D + 1e-5f);
  for (int j = 0; j < 8; ++j) {
    int col = t * 8 + j;
    h[(size_t)row * D + col] = f2bf((v[j] - m) * r * (1.f + scale[col]) + shift[col]);
  }
}

template <int MODE>
__global__ void k_gemm(const u16* A, const u16* B, const u16* B2, int N, int K, void* out, const void* resid, const float* gate) {
  int lane = threadIdx.x & 63;
  int wave = threadIdx.x >> 6;
  int tm = blockIdx.y * 4 + wave, tn = blockIdx.x;
  int r = lane & 15, q = lane >> 4;
  f32x4 acc = {0, 0, 0, 0}, acc2 = {0, 0, 0, 0};
  for (int k0 = 0; k0 < K; k0 += 32) {
    bf16x8 a = *(const bf16x8*)(A + (size_t)(tm * 16 + r) * K + k0 + q * 8);
    bf16x8 b, b2;
    for (int j = 0; j < 8; ++j) b[j] = (short)B[(size_t)(k0 + q * 8 + j) * N + tn * 16 + r];
    acc = __builtin_amdgcn_mfma_f32_16x16x32_bf16(a, b, acc, 0, 0, 0);
    if (MODE == 3) {
      for (int j = 0; j < 8; ++j) b2[j] = (short)B2[(size_t)(k0 + q * 8 + j) * N + tn * 16 + r];
      acc2 = __builtin_amdgcn_mfma_f32_16x16x32_bf16(a, b2, acc2, 0, 0, 0);
    }
  }
  for (int j = 0; j < 4; ++j) {
    int row = tm * 16 + q * 4 + j, col = tn * 16 + r;
    size_t idx = (size_t)row * N + col;
    if (MODE == 0) ((u16*)out)[idx] = f2bf(acc[j]);
    if (MODE == 1) ((float*)out)[idx] = rbf(bf2f(((const u16*)resid)[idx]) + gate[col] * acc[j]);
    if (MODE == 2) ((u16*)out)[idx] = f2bf(((const float*)resid)[idx] + gate[col] * acc[j]);
    if (MODE == 3) { float g = acc[j]; ((u16*)out)[idx] = f2bf(g / (1.f + __expf(-g)) * acc2[j]); }
  }
}

__global__ void k_attn(const u16* qkv, u16* o) {
  int idx = blockIdx.x * blockDim.x + threadIdx.x;
  int s = idx / H, h = idx % H;
  float q[DH];
  for (int d = 0; d < DH; ++d) q[d] = bf2f(qkv[(size_t)s * 1536 + h * DH + d]);
  float lg[8];
  float mx = -1e30f;
  for (int w = 0; w < 8; ++w) {
    int p = s - 2 * w;
    float a = -1e9f;
    if (p >= 0) {
      a = 0.f;
      for (int d = 0; d < DH; ++d) a += q[d] * bf2f(qkv[(size_t)p * 1536 + 512 + h * DH + d]);
      a *= 0.25f;
    }
    lg[w] = a;
    mx = fmaxf(mx, a);
  }
  float sum = 0.f;
  for (int w = 0; w < 8; ++w) { lg[w] = __expf(lg[w] - mx); sum += lg[w]; }
  float inv = 1.f / sum;
  float acc[DH];
  for (int d = 0; d < DH; ++d) acc[d] = 0.f;
  for (int w = 0; w < 8; ++w) {
    int p = s - 2 * w;
    if (p < 0) p = 0;
    float pw = lg[w] * inv;
    for (int d = 0; d < DH; ++d) acc[d] += pw * bf2f(qkv[(size_t)p * 1536 + 1024 + h * DH + d]);
  }
  for (int d = 0; d < DH; ++d) o[(size_t)s * D + h * DH + d] = f2bf(acc[d]);
}

extern "C" void kernel_launch(void* const* d_in, const int* in_sizes, int n_in, void* d_out, int out_size, void* d_ws,
                              size_t ws_size, hipStream_t stream) {
  const u16* x = (const u16*)d_in[0];
  const u16* wqkv = (const u16*)d_in[1];
  const u16* wo = (const u16*)d_in[2];
  const u16* w1 = (const u16*)d_in[4];
  const u16* w3 = (const u16*)d_in[5];
  const u16* w2 = (const u16*)d_in[6];
  const u16* c = (const u16*)d_in[7];
  const u16* wmod = (const u16*)d_in[8];
  char* ws = (char*)d_ws;
  float* mod = (float*)ws;
  u16* h1 = (u16*)(ws + (1 << 20));
  u16* qkv = (u16*)(ws + (4 << 20));
  u16* o = (u16*)(ws + (12 << 20));
  float* x1 = (float*)(ws + (16 << 20));
  u16* h2 = (u16*)(ws + (24 << 20));
  u16* hm = (u16*)(ws + (28 << 20));
  k_mod<<<3072 / 64, 64, 0, stream>>>(c, wmod, mod);
  k_ln<false><<<S, 64, 0, stream>>>(x, mod + 0, mod + 512, h1);
  k_gemm<0><<<dim3(1536 / 16, S / 64), 256, 0, stream>>>(h1, wqkv, nullptr, 1536, 512, qkv, nullptr, nullptr);
  k_attn<<<S * H / 256, 256, 0, stream>>>(qkv, o);
  k_gemm<1><<<dim3(512 / 16, S / 64), 256, 0, stream>>>(o, wo, nullptr, 512, 512, x1, x, mod + 1024);
  k_ln<true><<<S, 64, 0, stream>>>(x1, mod + 1536, mod + 2048, h2);
  k_gemm<3><<<dim3(F / 16, S / 64), 256, 0, stream>>>(h2, w1, w3, F, 512, hm, nullptr, nullptr);
  k_gemm<2><<<dim3(512 / 16, S / 64), 256, 0, stream>>>(hm, w2, nullptr, 512, F, d_out, x1, mod + 2560);
}
```

```cpp
#include <hip/hip_runtime.h>
#include <stdint.h>

namespace mk {
typedef unsigned short u16;
typedef unsigned u32;
typedef unsigned long long u64;
typedef u32 v4u __attribute__((ext_vector_type(4)));
typedef u32 v2u __attribute__((ext_vector_type(2)));
typedef float f32x4 __attribute__((ext_vector_type(4)));
typedef float f32x2 __attribute__((ext_vector_type(2)));
typedef __bf16 bf16x8 __attribute__((ext_vector_type(8)));
typedef __bf16 bf16x2 __attribute__((ext_vector_type(2)));
typedef short s16x4 __attribute__((ext_vector_type(4)));
#define GAS __attribute__((address_space(1)))
#define LAS __attribute__((address_space(3)))
#define DI __device__ __forceinline__
#define RLX_AGENT __ATOMIC_RELAXED, __HIP_MEMORY_SCOPE_AGENT
#define RLX_WG __ATOMIC_RELAXED, __HIP_MEMORY_SCOPE_WORKGROUP
#define CBAR() asm volatile("" ::: "memory")
#define DRAIN() asm volatile("s_waitcnt vmcnt(0)" ::: "memory")
#define LGKM0() asm volatile("s_waitcnt lgkmcnt(0)" ::: "memory")
#define SB0() __builtin_amdgcn_sched_barrier(0)

constexpr int S = 2048, D = 512, F = 1376;
constexpr int AUX_SC1 = 16;
constexpr u32 WS_EPOCH = 0, WS_ARR = 4096, WS_MOD = 8192, WS_FLAG = 65536;
constexpr u32 WS_O = 2u << 20, WS_X1 = 4u << 20, WS_HM = 6u << 20, WS_END = 12u << 20, WS_DBG = 16u << 20;
constexpr u32 HM_TILE = 176 * 256;
constexpr u32 L_TILE = 16384;
constexpr u32 L_STG = 81920, QSLOT = 24576, WSLOT = 24608, W3OFF = 12320;
constexpr u32 L_SCR = 131200;
constexpr u32 L_MISC = 155776;
constexpr u32 L_MOD1 = L_MISC;
constexpr u32 L_AS2 = L_MISC + 2048;
constexpr u32 L_GA = L_AS2 + 4096;
constexpr u32 L_GB = L_GA + 256;
constexpr u32 L_PART = L_GB + 256;
constexpr u32 L_PF = L_PART + 256;
constexpr u32 L_CNT = L_PF + 256;
constexpr u32 L_END = L_CNT + 256;
static_assert(L_END <= 163840, "LDS");
enum { C_TAG = 0, C_YBAR = 2, C_XBAR = 3, C_QLOW = 4, C_QALL = 5, C_MOD1 = 6, C_MOD2 = 7, C_H1 = 8  , C_P1DONE = 13, C_W13 = 14, C_PART = 15, C_H2 = 16  , C_RED = 24, C_REDD = 25, C_N = 32 };
constexpr u32 PF_BYTES = 40960;
constexpr u32 SPIN_G = 1u << 14;
constexpr u32 SPIN_L = 1u << 19;

DI u32 pk2(float a, float b) { f32x2 v = {a, b}; return __builtin_bit_cast(u32, __builtin_convertvector(v, bf16x2)); }
DI float lo16(u32 x) { return __builtin_bit_cast(float, x << 16); }
DI float hi16(u32 x) { return __builtin_bit_cast(float, x & 0xffff0000u); }
DI float rbf(float a) { return lo16(pk2(a, 0.f)); }
DI f32x4 mfma32(v4u a, v4u b, f32x4 c) { return __builtin_amdgcn_mfma_f32_16x16x32_bf16(__builtin_bit_cast(bf16x8, a), __builtin_bit_cast(bf16x8, b), c, 0, 0, 0); }
DI f32x4 mfma16(v2u a, v2u b, f32x4 c) { return __builtin_amdgcn_mfma_f32_16x16x16bf16_1k(__builtin_bit_cast(s16x4, a), __builtin_bit_cast(s16x4, b), c, 0, 0, 0); }
DI float dot2(u32 a, u32 b, float c) { return __builtin_amdgcn_fdot2_f32_bf16(__builtin_bit_cast(bf16x2, a), __builtin_bit_cast(bf16x2, b), c, false); }
template <int CTRL> DI float dpp(float x) { return __builtin_bit_cast(float, __builtin_amdgcn_mov_dpp(__builtin_bit_cast(int, x), CTRL, 0xf, 0xf, true)); }
#define ROR(n) (0x120 + (n))
DI u32 fu(float x) { return __builtin_bit_cast(u32, x); }
DI float uf(u32 x) { return __builtin_bit_cast(float, x); }
DI float x16_add(float x) { auto s = __builtin_amdgcn_permlane16_swap(fu(x), fu(x), false, false); return uf(s[0]) + uf(s[1]); }
DI float x32_add(float x) { auto s = __builtin_amdgcn_permlane32_swap(fu(x), fu(x), false, false); return uf(s[0]) + uf(s[1]); }
DI float x16_max(float x) { auto s = __builtin_amdgcn_permlane16_swap(fu(x), fu(x), false, false); return fmaxf(uf(s[0]), uf(s[1])); }
DI float x32_max(float x) { auto s = __builtin_amdgcn_permlane32_swap(fu(x), fu(x), false, false); return fmaxf(uf(s[0]), uf(s[1])); }
DI float fold32(float a, float b) { auto s = __builtin_amdgcn_permlane32_swap(fu(a), fu(b), false, false); return uf(s[0]) + uf(s[1]); }
DI float fold16(float a, float b) { auto s = __builtin_amdgcn_permlane16_swap(fu(a), fu(b), false, false); return uf(s[0]) + uf(s[1]); }

typedef LAS unsigned char* lptr;
DI v4u lds_r128(lptr L, u32 off) { return *(LAS v4u*)(L + off); }
DI void lds_w128(lptr L, u32 off, v4u v) { *(LAS v4u*)(L + off) = v; }
DI v2u lds_tr(lptr L, u32 off) { return __builtin_bit_cast(v2u, __builtin_amdgcn_ds_read_tr16_b64_v4i16((LAS s16x4*)(L + off))); }
DI void cnt_add(lptr L, int c, int lane) {
  CBAR();
  if (lane == 0) __hip_atomic_fetch_add((LAS u32*)(L + L_CNT) + c, 1u, RLX_WG);
  CBAR();
}
DI void cnt_wait(lptr L, int c, u32 target) {
  CBAR();
#pragma unroll 1
  for (u32 i = 0; i < SPIN_L; ++i) {
    u32 v = __builtin_amdgcn_readfirstlane(__hip_atomic_load((LAS u32*)(L + L_CNT) + c, RLX_WG));
    if (v >= target) break;
    __builtin_amdgcn_s_sleep(1);
  }
  CBAR();
}
DI u32 get_tag(lptr L) {
  cnt_wait(L, C_TAG + 1, 1u);
  return __builtin_amdgcn_readfirstlane(__hip_atomic_load((LAS u32*)(L + L_CNT) + C_TAG, RLX_WG));
}
DI void dma16(const void* sbase, u32 voff, u32 ldsaddr) {
  u32 keep;
  asm volatile("s_mov_b32 %0, m0\n\ts_mov_b32 m0, %3\n\ts_nop 0\n\tglobal_load_lds_dwordx4 %1, %2\n\ts_mov_b32 m0, %0" : "=&s"(keep) : "v"(voff), "s"(sbase), "s"(ldsaddr) : "memory");
}
DI void dma4(const void* sbase, u32 voff, u32 ldsaddr) {
  u32 keep;
  asm volatile("s_mov_b32 %0, m0\n\ts_mov_b32 m0, %3\n\ts_nop 0\n\tglobal_load_lds_dword %1, %2\n\ts_mov_b32 m0, %0" : "=&s"(keep) : "v"(voff), "s"(sbase), "s"(ldsaddr) : "memory");
}
template <int N> DI void vmwait() { asm volatile("s_waitcnt vmcnt(%0)" ::"n"(N) : "memory"); }

DI u32 flag_off(int slot, int wg, int w) { return WS_FLAG + (u32)(((slot * 256 + wg) * 4 + w) * 128); }
DI void set_flag(unsigned char* ws, u32 off, u32 tag, int lane) {
  if (lane == 0) __hip_atomic_store((GAS u32*)(ws + off), tag, RLX_AGENT);
}
DI void poll_flags(unsigned char* ws, u32 off, u32 tag) {
#pragma unroll 1
  for (u32 i = 0; i < SPIN_G; ++i) {
    u32 f = __hip_atomic_load((GAS u32*)(ws + off), RLX_AGENT);
    if (__all(f == tag)) break;
    __builtin_amdgcn_s_sleep(4);
  }
  __builtin_amdgcn_fence(__ATOMIC_ACQUIRE, "wavefront");
  CBAR();
}
#define STAMP(i) do { } while (0)

__global__ void __launch_bounds__(512, 2)
mega(const u16* __restrict__ x, const u16* __restrict__ wqkv, const u16* __restrict__ wo, const u16* __restrict__ w1, const u16* __restrict__ w3,
     const u16* __restrict__ w2, const u16* __restrict__ cvec, const u16* __restrict__ wmod, u16* __restrict__ out, unsigned char* __restrict__ ws) {
  __shared__ __attribute__((aligned(16))) unsigned char smem[163840];
  lptr L = (lptr)smem;
  const u32 lbase = (u32)(size_t)L;
  const int tid = threadIdx.x, lane = tid & 63, wave = __builtin_amdgcn_readfirstlane(tid >> 6), w = wave & 3;
  const int bid = blockIdx.x;
  const int cl = (bid & 7) * 2 + (bid >> 7), r = (bid >> 3) & 15, wg = cl * 16 + r;
  const int tok = lane & 15, q = lane >> 4;
  const int bl = 2 * cl + (r >> 3);
  const int fs = r & 7;
  const __amdgpu_buffer_rsrc_t rws = __builtin_amdgcn_make_buffer_rsrc((void*)ws, 0, (int)WS_END, 0x00020000);
  STAMP(0);
  {
    u64 pc; asm volatile("s_getpc_b64 %0" : "=s"(pc));
    const u32 po = (u32)tid * 128u;
    dma4((const void*)(pc & ~127ull), po < PF_BYTES ? po : 0u, lbase + L_PF);
  }
  if (tid < C_N) ((LAS u32*)(L + L_CNT))[tid] = 0u;
  __syncthreads();

  if (wave < 4) {
    u32 e0 = 0;
    if (w == 0) e0 = __hip_atomic_load((GAS u32*)(ws + WS_EPOCH), RLX_AGENT);
    const bool prod = bid < 192;
    const int cg = bid < 64 ? (bid & 7) * 8 + (bid >> 3) : 64 + ((bid - 64) & 7) * 16 + ((bid - 64) >> 3);
    v4u wm[4]; v2u cv = {0u, 0u};
    if (prod) {
      const int row0 = 128 * w + 4 * (lane >> 1);
      cv = *(const GAS v2u*)(cvec + row0);
#pragma unroll
      for (int i = 0; i < 4; ++i) wm[i] = *(const GAS v4u*)(wmod + (size_t)(row0 + i) * 3072 + cg * 16 + (lane & 1) * 8);
    }
    v4u xr[20];
#pragma unroll
    for (int ti = 0; ti < 5; ++ti)
#pragma unroll
      for (int i = 0; i < 4; ++i) {
        int t = 64 * bl - 16 + 16 * ti + 4 * w + i; t = t < 0 ? 0 : t;
        xr[ti * 4 + i] = *(const GAS v4u*)(x + (size_t)t * D + lane * 8);
      }
    SB0();
    u32 tag = 0;
    if (w == 0) {
      tag = __builtin_amdgcn_readfirstlane(e0) * 1664525u + 1013904223u;
      if (lane == 0) {
        *(LAS v2u*)(L + L_CNT + 4 * C_TAG) = v2u{tag, 1u};
        __hip_atomic_store((GAS u32*)(ws + WS_ARR) + bid, tag, RLX_AGENT);
      }
      CBAR();
    }
    if (prod) {
      float sc[4];
      { const float c0 = lo16(cv.x), c1 = hi16(cv.x), c2 = lo16(cv.y), c3 = hi16(cv.y);
        sc[0] = rbf(c0 / (1.f + __expf(-c0))); sc[1] = rbf(c1 / (1.f + __expf(-c1))); sc[2] = rbf(c2 / (1.f + __expf(-c2))); sc[3] = rbf(c3 / (1.f + __expf(-c3))); }
      float a[8];
#pragma unroll
      for (int j = 0; j < 8; ++j) a[j] = 0.f;
#pragma unroll
      for (int i = 0; i < 4; ++i)
#pragma unroll
        for (int d = 0; d < 4; ++d) { a[2 * d] += sc[i] * lo16(wm[i][d]); a[2 * d + 1] += sc[i] * hi16(wm[i][d]); }
      float b0 = fold32(a[0], a[4]), b1 = fold32(a[1], a[5]), b2 = fold32(a[2], a[6]), b3 = fold32(a[3], a[7]);
      float c0 = fold16(b0, b2), c1 = fold16(b1, b3);
      c0 += dpp<ROR(8)>(c0); c1 += dpp<ROR(8)>(c1);
      c0 += dpp<ROR(4)>(c0); c1 += dpp<ROR(4)>(c1);
      c0 += dpp<ROR(2)>(c0); c1 += dpp<ROR(2)>(c1);
      if (tok < 2) *(LAS f32x2*)(L + L_PART + w * 64 + ((tok & 1) * 8 + (q & 1) * 2 + (q >> 1) * 4) * 4) = f32x2{c0, c1};
      LGKM0();
      cnt_add(L, C_PART, lane);
      if (w == 0) {
        cnt_wait(L, C_PART, 4u);
        if (lane < 8) {
          f32x2 s = {0.f, 0.f};
#pragma unroll
          for (int ww = 0; ww < 4; ++ww) s += *(LAS f32x2*)(L + L_PART + ww * 64 + lane * 8);
          __hip_atomic_store((GAS u64*)(ws + WS_MOD) + cg * 8 + lane, ((u64)tag << 32) | pk2(s.x, s.y), RLX_AGENT);
        }
      }
    }
    STAMP(1);
    float z[20][8];
#pragma unroll
    for (int ti = 0; ti < 5; ++ti) {
      float sm[4], sq[4];
#pragma unroll
      for (int i = 0; i < 4; ++i) {
        sm[i] = 0.f; sq[i] = 0.f;
#pragma unroll
        for (int d = 0; d < 4; ++d) { sm[i] = dot2(xr[ti * 4 + i][d], 0x3f803f80u, sm[i]); sq[i] = dot2(xr[ti * 4 + i][d], xr[ti * 4 + i][d], sq[i]); }
      }
      float us = fold16(fold32(sm[0], sm[1]), fold32(sm[2], sm[3]));
      float uq = fold16(fold32(sq[0], sq[1]), fold32(sq[2], sq[3]));
      us += dpp<ROR(8)>(us); uq += dpp<ROR(8)>(uq);
      us += dpp<ROR(4)>(us); uq += dpp<ROR(4)>(uq);
      us += dpp<ROR(2)>(us); uq += dpp<ROR(2)>(uq);
      us += dpp<ROR(1)>(us); uq += dpp<ROR(1)>(uq);
      const float mean = us * (1.f / D);
      const float var = fmaxf(uq * (1.f / D) - mean * mean, 0.f);
      const float rs = __builtin_amdgcn_rsqf(var + 1e-5f);
      const float nb = -mean * rs;
#pragma unroll
      for (int i = 0; i < 4; ++i) {
        const int ln = (i & 1) * 32 + (i >> 1) * 16;
        const float rsi = uf(__builtin_amdgcn_readlane(fu(rs), ln)), nbi = uf(__builtin_amdgcn_readlane(fu(nb), ln));
#pragma unroll
        for (int d = 0; d < 4; ++d) { z[ti * 4 + i][2 * d] = lo16(xr[ti * 4 + i][d]) * rsi + nbi; z[ti * 4 + i][2 * d + 1] = hi16(xr[ti * 4 + i][d]) * rsi + nbi; }
      }
    }
    STAMP(2);
    if (w != 0) tag = get_tag(L);
    if (w == 1) {
      v4u g[4];
#pragma unroll 1
      for (u32 it = 0; it < SPIN_G; ++it) {
        CBAR();
#pragma unroll
        for (int i = 0; i < 4; ++i) g[i] = __builtin_amdgcn_raw_buffer_load_b128(rws, WS_MOD + (64 * i + lane) * 16, 0, AUX_SC1);
        bool ok = true;
#pragma unroll
        for (int i = 0; i < 4; ++i) ok = ok && g[i].y == tag && g[i].w == tag;
        if (__all(ok)) break;
        __builtin_amdgcn_s_sleep(6);
      }
#pragma unroll
      for (int i = 0; i < 4; ++i) *(LAS v2u*)(L + L_MOD1 + (64 * i + lane) * 8) = v2u{g[i].x, g[i].z};
      LGKM0();
      cnt_add(L, C_MOD1, lane);
    }
    cnt_wait(L, C_MOD1, 1u);
    STAMP(3);
    {
      const v4u sav = lds_r128(L, L_MOD1 + lane * 16), cav = lds_r128(L, L_MOD1 + 1024 + lane * 16);
      float a1[8], s1[8];
#pragma unroll
      for (int d = 0; d < 4; ++d) { s1[2 * d] = lo16(sav[d]); s1[2 * d + 1] = hi16(sav[d]); a1[2 * d] = rbf(1.f + lo16(cav[d])); a1[2 * d + 1] = rbf(1.f + hi16(cav[d])); }
      cnt_wait(L, C_QLOW, 4u);
      STAMP(4);
#pragma unroll
      for (int ti = 0; ti < 5; ++ti) {
#pragma unroll
        for (int i = 0; i < 4; ++i) {
          v4u h;
#pragma unroll
          for (int d = 0; d < 4; ++d) h[d] = pk2(z[ti * 4 + i][2 * d] * a1[2 * d] + s1[2 * d], z[ti * 4 + i][2 * d + 1] * a1[2 * d + 1] + s1[2 * d + 1]);
          lds_w128(L, ti * L_TILE + lane * 256 + (((4 * w + i) ^ (lane & 7)) * 16), h);
        }
        LGKM0();
        cnt_add(L, C_H1 + ti, lane);
      }
    }
    STAMP(5);
    if (w == 2) {
      v4u g[4]; v2u ga, gb;
#pragma unroll 1
      for (u32 it = 0; it < SPIN_G; ++it) {
        CBAR();
#pragma unroll
        for (int i = 0; i < 4; ++i) g[i] = __builtin_amdgcn_raw_buffer_load_b128(rws, WS_MOD + 768 * 8 + (64 * i + lane) * 16, 0, AUX_SC1);
        ga = __builtin_amdgcn_raw_buffer_load_b64(rws, WS_MOD + (512 + fs * 32 + (lane & 31)) * 8, 0, AUX_SC1);
        gb = __builtin_amdgcn_raw_buffer_load_b64(rws, WS_MOD + (1280 + fs * 32 + (lane & 31)) * 8, 0, AUX_SC1);
        bool ok = ga.y == tag && gb.y == tag;
#pragma unroll
        for (int i = 0; i < 4; ++i) ok = ok && g[i].y == tag && g[i].w == tag;
        if (__all(ok)) break;
        __builtin_amdgcn_s_sleep(6);
      }
#pragma unroll
      for (int i = 0; i < 2; ++i) {
        const int k = 4 * (64 * i + lane);
        *(LAS f32x4*)(L + L_AS2 + (k >> 3) * 64 + 32 + (k & 4) * 4) = f32x4{lo16(g[i].x), hi16(g[i].x), lo16(g[i].z), hi16(g[i].z)};
        *(LAS f32x4*)(L + L_AS2 + (k >> 3) * 64 + (k & 4) * 4) =
            f32x4{rbf(1.f + lo16(g[2 + i].x)), rbf(1.f + hi16(g[2 + i].x)), rbf(1.f + lo16(g[2 + i].z)), rbf(1.f + hi16(g[2 + i].z))};
      }
      if (lane < 32) { *(LAS f32x2*)(L + L_GA + lane * 8) = f32x2{lo16(ga.x), hi16(ga.x)}; *(LAS f32x2*)(L + L_GB + lane * 8) = f32x2{lo16(gb.x), hi16(gb.x)}; }
      LGKM0();
      cnt_add(L, C_MOD2, lane);
    }
    if (w == 3 && bid == 0) {
#pragma unroll 1
      for (u32 it = 0; it < SPIN_G; ++it) {
        CBAR();
        const v4u a = __builtin_amdgcn_raw_buffer_load_b128(rws, WS_ARR + lane * 16, 0, AUX_SC1);
        if (__all(a.x == tag && a.y == tag && a.z == tag && a.w == tag)) break;
        __builtin_amdgcn_s_sleep(32);
      }
      if (lane == 0) __hip_atomic_store((GAS u32*)(ws + WS_EPOCH), tag, RLX_AGENT);
    }
    const int v = 4 * r + w;
    v4u W[3][16];
    cnt_wait(L, C_QALL, 4u);
    STAMP(6);
    if (r < 15) {
      u32 voff[3];
#pragma unroll
      for (int n = 0; n < 3; ++n) {
        const int Lx = 64 * n + lane, rl = Lx / 12, pp = (Lx - 12 * rl) ^ (w & 1);
        int col = 96 * r + 8 * pp; col = col < F ? col : 0;
        voff[n] = (u32)(((16 * w + rl) * F + col) * 2);
      }
      u32 tb[3];
#pragma unroll
      for (int t = 0; t < 3; ++t)
        tb[t] = L_STG + ((lane & 3) >> 1) * W3OFF + (16 * q + ((lane & 15) >> 2)) * 192 + (((3 * w + t) ^ (q & 1)) * 16) + (lane & 1) * 8;
      auto issue = [&](int ch) {
        const u32 dst = lbase + L_STG + (ch & 1) * WSLOT + 16 * w * 192;
#pragma unroll
        for (int n = 0; n < 3; ++n) { dma16(w1 + (size_t)ch * 64 * F, voff[n], dst + n * 1024); dma16(w3 + (size_t)ch * 64 * F, voff[n], dst + W3OFF + n * 1024); }
      };
      DRAIN();
      issue(0); issue(1);
#pragma unroll
      for (int ch = 0; ch < 8; ++ch) {
        if (ch < 7) vmwait<6>(); else vmwait<0>();
        cnt_add(L, C_XBAR, lane); cnt_wait(L, C_XBAR, 4u * (2 * ch + 1));
#pragma unroll
        for (int sl = 0; sl < 2; ++sl)
#pragma unroll
          for (int t = 0; t < 3; ++t) {
            const v2u a = lds_tr(L, tb[t] + (ch & 1) * WSLOT + (8 * sl) * 192), b = lds_tr(L, tb[t] + (ch & 1) * WSLOT + (8 * sl + 4) * 192);
            W[t][2 * ch + sl] = v4u{a.x, a.y, b.x, b.y};
          }
        LGKM0();
#pragma unroll
        for (int sl = 0; sl < 2; ++sl)
#pragma unroll
          for (int t = 0; t < 3; ++t) asm volatile("" : "+v"(W[t][2 * ch + sl]));
        cnt_add(L, C_XBAR, lane); cnt_wait(L, C_XBAR, 4u * (2 * ch + 2));
        if (ch + 2 < 8) issue(ch + 2);
      }
    } else {
#pragma unroll
      for (int t = 0; t < 3; ++t)
#pragma unroll
        for (int s = 0; s < 16; ++s) W[t][s] = v4u{0u, 0u, 0u, 0u};
    }
    cnt_add(L, C_W13, lane);
    STAMP(7);
    if (24 * v < F) {
#pragma unroll 1
      for (int it = 0; it < 8; ++it) {
        cnt_wait(L, C_H2 + it, 1u);
        if (it == 0) STAMP(8);
        if (it == 4) STAMP(10);
        f32x4 acc[3] = {{0.f, 0.f, 0.f, 0.f}, {0.f, 0.f, 0.f, 0.f}, {0.f, 0.f, 0.f, 0.f}};
        const u32 hb = it * L_TILE + lane * 16;
#pragma unroll
        for (int s = 0; s < 16; ++s) {
          const v4u b = lds_r128(L, hb + s * 1024);
#pragma unroll
          for (int t = 0; t < 3; ++t) acc[t] = mfma32(W[t][s], b, acc[t]);
        }
        u32 dw[3];
#pragma unroll
        for (int t = 0; t < 3; ++t) {
          auto s0 = __builtin_amdgcn_permlane32_swap(fu(acc[t][0]), fu(acc[t][2]), false, false);
          auto s1 = __builtin_amdgcn_permlane32_swap(fu(acc[t][1]), fu(acc[t][3]), false, false);
          const float g0 = uf(s0[0]), u0 = uf(s0[1]), g1 = uf(s1[0]), u1 = uf(s1[1]);
          const float h0 = g0 * u0 * __builtin_amdgcn_rcpf(1.f + __expf(-g0)), h1 = g1 * u1 * __builtin_amdgcn_rcpf(1.f + __expf(-g1));
          dw[t] = (24 * v + 8 * t < F) ? pk2(h0, h1) : 0u;
        }
        const u32 e0_ = __builtin_amdgcn_ds_bpermute((48 + tok) * 4, dw[0]), e1_ = __builtin_amdgcn_ds_bpermute((48 + tok) * 4, dw[1]), e2_ = __builtin_amdgcn_ds_bpermute((48 + tok) * 4, dw[2]);
        const u32 ex = q == 0 ? e0_ : (q == 1 ? e1_ : e2_);
        const int ul = 2 * ((it & 3) >> 1) + (it >> 2), tile = 8 * cl + 2 * ul + (it & 1);
        if (q < 3) __builtin_amdgcn_raw_buffer_store_b128(v4u{dw[0], dw[1], dw[2], ex}, rws, WS_HM + tile * HM_TILE + v * 768 + lane * 16, 0, AUX_SC1);
        if (it & 1) { DRAIN(); set_flag(ws, flag_off(4 + ul, wg, w), tag, lane); }
        if (it == 3) STAMP(9);
      }
    }
    STAMP(11);
  } else {
    __builtin_amdgcn_s_setprio(1);
    v4u W[3][16];
    {
      u32 voff[2];
#pragma unroll
      for (int hh = 0; hh < 2; ++hh) {
        const int R = 16 * w + 8 * hh + (lane >> 3), g = ((lane >> 4) & 1) | (hh << 1), pos = lane & 7;
        voff[hh] = (u32)((R * 1536 + 64 * fs + ((pos >> 1) ^ g) * 16 + (pos & 1) * 8) * 2);
      }
      auto slot = [&](int ch) -> u32 { const int sidx = ch < 6 ? ch : ch - 6; return sidx < 3 ? L_STG + sidx * QSLOT : (sidx - 3) * QSLOT; };
      auto issue = [&](int ch) {
#pragma unroll
        for (int m = 0; m < 3; ++m)
#pragma unroll
          for (int hh = 0; hh < 2; ++hh) dma16(wqkv + (size_t)ch * 64 * 1536 + m * 512, voff[hh], lbase + slot(ch) + m * 8192 + (16 * w + 8 * hh) * 128);
      };
#pragma unroll
      for (int ch = 0; ch < 6; ++ch) issue(ch);
      const int i4 = (lane & 15) >> 2;
      const u32 tb = (8 * q + i4) * 128 + ((w ^ ((i4 >> 1) | ((q & 1) << 1))) * 32) + (lane & 3) * 8;
#pragma unroll
      for (int ch = 0; ch < 8; ++ch) {
        if (ch == 0) vmwait<30>(); else if (ch < 4) vmwait<24>(); else if (ch == 4) vmwait<18>(); else if (ch == 5) vmwait<12>(); else if (ch == 6) vmwait<6>(); else vmwait<0>();
        cnt_add(L, C_YBAR, lane); cnt_wait(L, C_YBAR, 4u * (ch + 1));
        if (ch == 1 || ch == 2) issue(ch + 5);
#pragma unroll
        for (int sl = 0; sl < 2; ++sl)
#pragma unroll
          for (int m = 0; m < 3; ++m) {
            const v2u a = lds_tr(L, tb + slot(ch) + m * 8192 + sl * 4096), b = lds_tr(L, tb + slot(ch) + m * 8192 + sl * 4096 + 512);
            W[m][2 * ch + sl] = v4u{a.x, a.y, b.x, b.y};
          }
        LGKM0();
#pragma unroll
        for (int sl = 0; sl < 2; ++sl)
#pragma unroll
          for (int m = 0; m < 3; ++m) asm volatile("" : "+v"(W[m][2 * ch + sl]));
        if (ch == 5) cnt_add(L, C_QLOW, lane);
        if (ch == 7) cnt_add(L, C_QALL, lane);
      }
    }
    STAMP(1);
    const u32 tag = get_tag(L);
    {
      const u32 be = q * 256 + ((tok ^ q) * 16), bo = q * 256 + ((tok ^ (4 + q)) * 16);
      v2u kp, vp;
      cnt_wait(L, C_H1, 4u);
      STAMP(2);
      {
        f32x4 ka = {0.f, 0.f, 0.f, 0.f}, va = {0.f, 0.f, 0.f, 0.f};
#pragma unroll
        for (int s = 0; s < 16; ++s) {
          const v4u b = lds_r128(L, ((s & 1) ? bo : be) + s * 1024);
          ka = mfma32(W[1][s], b, ka); va = mfma32(b, W[2][s], va);
        }
        kp = v2u{pk2(ka[0], ka[1]), pk2(ka[2], ka[3])}; vp = v2u{pk2(va[0], va[1]), pk2(va[2], va[3])};
      }
      const bool par = tok & 1;
      const bool cur0 = 4 * q + (tok & 1) <= tok, cur1 = 4 * q + 2 + (tok & 1) <= tok;
      v2u osave = {0u, 0u};
#pragma unroll 1
      for (int ti = 1; ti < 5; ++ti) {
        cnt_wait(L, C_H1 + ti, 4u);
        f32x4 qa = {0.f, 0.f, 0.f, 0.f}, ka = {0.f, 0.f, 0.f, 0.f}, va = {0.f, 0.f, 0.f, 0.f};
#pragma unroll
        for (int s = 0; s < 16; ++s) {
          const v4u b = lds_r128(L, ti * L_TILE + ((s & 1) ? bo : be) + s * 1024);
          qa = mfma32(W[0][s], b, qa); ka = mfma32(W[1][s], b, ka); va = mfma32(b, W[2][s], va);
        }
        const v2u qc = {pk2(qa[0], qa[1]), pk2(qa[2], qa[3])}, kc = {pk2(ka[0], ka[1]), pk2(ka[2], ka[3])}, vc = {pk2(va[0], va[1]), pk2(va[2], va[3])};
        const f32x4 zero = {0.f, 0.f, 0.f, 0.f};
        const f32x4 sc = mfma16(kc, qc, zero), sp = mfma16(kp, qc, zero);
        const bool noprev = (bl == 0 && ti == 1);
        const float NEG = -1e30f;
        float p0 = par ? sp[1] : sp[0], p1 = par ? sp[3] : sp[2];
        if (noprev) { p0 = NEG; p1 = NEG; }
        const float l0 = cur0 ? (par ? sc[1] : sc[0]) : p0, l1 = cur1 ? (par ? sc[3] : sc[2]) : p1;
        const float mx = x32_max(x16_max(fmaxf(l0, l1)));
        const float CS = 0.25f * 1.4426950408889634f;
        const float e0 = __builtin_amdgcn_exp2f((l0 - mx) * CS), e1 = __builtin_amdgcn_exp2f((l1 - mx) * CS);
        const float inv = __builtin_amdgcn_rcpf(x32_add(x16_add(e0 + e1)));
        const u32 b0 = pk2(e0 * inv, 0.f), b1 = pk2(e1 * inv, 0.f);
        const u32 d0 = par ? b0 << 16 : b0, d1 = par ? b1 << 16 : b1;
        const v4u pb = {cur0 ? 0u : d0, cur1 ? 0u : d1, cur0 ? d0 : 0u, cur1 ? d1 : 0u};
        const f32x4 o = mfma32(v4u{vp.x, vp.y, vc.x, vc.y}, pb, zero);
        const v2u ob = {pk2(o[0], o[1]), pk2(o[2], o[3])};
        kp = kc; vp = vc;
        if (ti & 1) osave = ob;
        else {
          const int st = (ti >> 1) - 1, u = 2 * bl + st;
          __builtin_amdgcn_raw_buffer_store_b128(v4u{osave.x, osave.y, ob.x, ob.y}, rws, WS_O + (u * 32 + 4 * fs + w) * 1024 + lane * 16, 0, AUX_SC1);
          DRAIN(); set_flag(ws, flag_off(st, wg, w), tag, lane);
          if (ti == 2) STAMP(3);
        }
      }
      cnt_add(L, C_P1DONE, lane);
    }
    STAMP(4);
    const int n0 = 64 * fs;
    const int rho = tok ^ (2 * (w & 1));
    const int wgb = cl * 16 + (r & 8);
    v2u x1s[2][2];
    u32 redn = 0;
    auto reduce = [&](f32x4 (&acc)[2][4], float (&res)[8]) {
      cnt_wait(L, C_REDD, 4u * redn);
#pragma unroll
      for (int pl = 1; pl < 4; ++pl) {
        const int tt = pl >> 1, hf = pl & 1;
        const u32 dst = L_SCR + ((w ^ pl) * 3 + pl - 1) * 2048 + lane * 16;
        *(LAS f32x4*)(L + dst) = f32x4{acc[tt][0][2 * hf], acc[tt][1][2 * hf], acc[tt][2][2 * hf], acc[tt][3][2 * hf]};
        *(LAS f32x4*)(L + dst + 1024) = f32x4{acc[tt][0][2 * hf + 1], acc[tt][1][2 * hf + 1], acc[tt][2][2 * hf + 1], acc[tt][3][2 * hf + 1]};
      }
      LGKM0();
      ++redn;
      cnt_add(L, C_RED, lane); cnt_wait(L, C_RED, 4u * redn);
#pragma unroll
      for (int t = 0; t < 4; ++t) { res[t] = acc[0][t][0]; res[4 + t] = acc[0][t][1]; }
#pragma unroll
      for (int pl = 0; pl < 3; ++pl) {
        const f32x4 a = *(LAS f32x4*)(L + L_SCR + (w * 3 + pl) * 2048 + lane * 16), b = *(LAS f32x4*)(L + L_SCR + (w * 3 + pl) * 2048 + 1024 + lane * 16);
#pragma unroll
        for (int t = 0; t < 4; ++t) { res[t] += a[t]; res[4 + t] += b[t]; }
      }
      LGKM0();
#pragma unroll
      for (int t = 0; t < 8; ++t) asm volatile("" : "+v"(res[t]));
      cnt_add(L, C_REDD, lane);
    };
    const int fcol = n0 + 16 * q + 8 * (w & 1);
    {
      v4u Wo[4][4];
#pragma unroll
      for (int i = 0; i < 4; ++i) {
        v2u raw[8];
#pragma unroll
        for (int jj = 0; jj < 8; ++jj) raw[jj] = *(const GAS v2u*)(wo + (size_t)((2 * (4 * w + i) + (jj >> 2)) * 16 + 4 * q + (jj & 3)) * D + n0 + 4 * rho);
#pragma unroll
        for (int t = 0; t < 4; ++t)
#pragma unroll
          for (int d = 0; d < 4; ++d) Wo[i][t][d] = __builtin_amdgcn_perm(raw[2 * d + 1][t >> 1], raw[2 * d][t >> 1], (t & 1) ? 0x07060302u : 0x05040100u);
      }
      cnt_wait(L, C_MOD2, 1u);
      cnt_wait(L, C_P1DONE, 4u);
#pragma unroll
      for (int st = 0; st < 2; ++st) {
        const int u = 2 * bl + st;
        const int mytile = 2 * u + (w >> 1);
        const v4u xres = *(const GAS v4u*)(x + (size_t)(mytile * 16 + tok) * D + fcol);
        poll_flags(ws, flag_off(st, wgb + 2 * w + ((lane >> 2) & 1), lane & 3), tag);
        if (st == 0) STAMP(5);
        v4u o[8];
#pragma unroll
        for (int h = 0; h < 8; ++h) o[h] = __builtin_amdgcn_raw_buffer_load_b128(rws, WS_O + (u * 32 + 8 * w + h) * 1024 + lane * 16, 0, AUX_SC1);
        f32x4 acc[2][4];
#pragma unroll
        for (int tt = 0; tt < 2; ++tt)
#pragma unroll
          for (int t = 0; t < 4; ++t) acc[tt][t] = f32x4{0.f, 0.f, 0.f, 0.f};
        const bool sw = (w >> 1) & 1;
#pragma unroll
        for (int i = 0; i < 4; ++i) {
          const v4u f0 = {o[2 * i].x, o[2 * i].y, o[2 * i + 1].x, o[2 * i + 1].y}, f1 = {o[2 * i].z, o[2 * i].w, o[2 * i + 1].z, o[2 * i + 1].w};
          const v4u fa = sw ? f1 : f0, fb = sw ? f0 : f1;
#pragma unroll
          for (int t = 0; t < 4; ++t) { acc[0][t] = mfma32(Wo[i][t], fa, acc[0][t]); acc[1][t] = mfma32(Wo[i][t], fb, acc[1][t]); }
        }
        float res[8];
        reduce(acc, res);
        const f32x4 g0 = *(LAS f32x4*)(L + L_GA + (fcol - n0) * 4), g1 = *(LAS f32x4*)(L + L_GA + (fcol - n0) * 4 + 16);
        v4u y;
        y.x = pk2(lo16(xres.x) + g0[0] * res[0], hi16(xres.x) + g0[1] * res[1]);
        y.y = pk2(lo16(xres.y) + g0[2] * res[2], hi16(xres.y) + g0[3] * res[3]);
        y.z = pk2(lo16(xres.z) + g1[0] * res[4], hi16(xres.z) + g1[1] * res[5]);
        y.w = pk2(lo16(xres.w) + g1[2] * res[6], hi16(xres.w) + g1[3] * res[7]);
        x1s[st][0] = v2u{y.x, y.y}; x1s[st][1] = v2u{y.z, y.w};
        __builtin_amdgcn_raw_buffer_store_b128(y, rws, WS_X1 + (mytile * 16 + 2 * fs + (w & 1)) * 1024 + lane * 16, 0, AUX_SC1);
        DRAIN(); set_flag(ws, flag_off(2 + st, wg, w), tag, lane);
        if (st == 0) STAMP(6);
      }
    }
    STAMP(7);
#pragma unroll 1
    for (int gp = 0; gp < 2; ++gp) {
      const int ul = 2 * (w >> 1) + gp, tile = 8 * cl + 2 * ul + (w & 1);
      poll_flags(ws, flag_off(2 + gp, cl * 16 + (w >> 1) * 8 + ((lane >> 1) & 7), 2 * (w & 1) + (lane & 1)), tag);
      if (gp == 0) STAMP(8);
      v4u xf[16];
#pragma unroll
      for (int s = 0; s < 16; ++s) xf[s] = __builtin_amdgcn_raw_buffer_load_b128(rws, WS_X1 + (tile * 16 + s) * 1024 + lane * 16, 0, AUX_SC1);
      float sm = 0.f, sq = 0.f;
#pragma unroll
      for (int s = 0; s < 16; ++s)
#pragma unroll
        for (int d = 0; d < 4; ++d) { sm = dot2(xf[s][d], 0x3f803f80u, sm); sq = dot2(xf[s][d], xf[s][d], sq); }
      sm = x32_add(x16_add(sm)); sq = x32_add(x16_add(sq));
      const float mean = sm * (1.f / D);
      const float var = fmaxf(sq * (1.f / D) - mean * mean, 0.f);
      const float rs = __builtin_amdgcn_rsqf(var + 1e-5f), nb = -mean * rs;
      if (gp == 1) cnt_wait(L, C_W13, 4u);
#pragma unroll
      for (int s = 0; s < 16; ++s) {
        const u32 ab = L_AS2 + q * 128 + (s >> 1) * 512 + (s & 1) * 64;
        const f32x4 a0 = *(LAS f32x4*)(L + ab), a1 = *(LAS f32x4*)(L + ab + 16), s0 = *(LAS f32x4*)(L + ab + 32), s1 = *(LAS f32x4*)(L + ab + 48);
        v4u h;
        h.x = pk2((lo16(xf[s].x) * rs + nb) * a0[0] + s0[0], (hi16(xf[s].x) * rs + nb) * a0[1] + s0[1]);
        h.y = pk2((lo16(xf[s].y) * rs + nb) * a0[2] + s0[2], (hi16(xf[s].y) * rs + nb) * a0[3] + s0[3]);
        h.z = pk2((lo16(xf[s].z) * rs + nb) * a1[0] + s1[0], (hi16(xf[s].z) * rs + nb) * a1[1] + s1[1]);
        h.w = pk2((lo16(xf[s].w) * rs + nb) * a1[2] + s1[2], (hi16(xf[s].w) * rs + nb) * a1[3] + s1[3]);
        lds_w128(L, (gp * 4 + w) * L_TILE + s * 1024 + lane * 16, h);
      }
      LGKM0();
      cnt_add(L, C_H2 + gp * 4 + w, lane);
      if (gp == 0) STAMP(9);
    }
    STAMP(10);
    {
      const __amdgpu_buffer_rsrc_t rw2 = __builtin_amdgcn_make_buffer_rsrc((void*)w2, 0, F * D * 2, 0x00020000);
      v4u W2[11][4];
#pragma unroll
      for (int kk = 0; kk < 11; ++kk) {
        const int gm = 44 * w + 4 * kk + q, vv = gm / 3, g = gm - 3 * vv;
        const u32 cb = (u32)((n0 + 4 * rho) * 2);
        const u32 ra = (u32)(24 * vv + 4 * (g & 1) + 2 * (g >> 1)) * 1024 + cb, rb = (u32)(24 * vv + 8 * g + 6) * 1024 + cb;
        v2u raw[8];
#pragma unroll
        for (int jj = 0; jj < 6; ++jj) raw[jj] = __builtin_amdgcn_raw_buffer_load_b64(rw2, ra + (jj & 1) * 1024, (jj >> 1) * 8192, 0);
        raw[6] = __builtin_amdgcn_raw_buffer_load_b64(rw2, rb, 0, 0);
        raw[7] = __builtin_amdgcn_raw_buffer_load_b64(rw2, rb + 1024, 0, 0);
#pragma unroll
        for (int t = 0; t < 4; ++t)
#pragma unroll
          for (int d = 0; d < 4; ++d) W2[kk][t][d] = __builtin_amdgcn_perm(raw[2 * d + 1][t >> 1], raw[2 * d][t >> 1], (t & 1) ? 0x07060302u : 0x05040100u);
      }
      STAMP(11);
      int vpoll = (44 * w) / 3 + (lane & 15); vpoll = vpoll > (44 * w + 43) / 3 ? (44 * w + 43) / 3 : vpoll; vpoll = vpoll > 57 ? 57 : vpoll;
#pragma unroll 1
      for (int st = 0; st < 2; ++st) {
        const int ul = 2 * (r >> 3) + st, u = 4 * cl + ul;
        poll_flags(ws, flag_off(4 + ul, cl * 16 + (vpoll >> 2), vpoll & 3), tag);
        if (st == 0) STAMP(12); else STAMP(14);
        f32x4 acc[2][4];
#pragma unroll
        for (int tt = 0; tt < 2; ++tt)
#pragma unroll
          for (int t = 0; t < 4; ++t) acc[tt][t] = f32x4{0.f, 0.f, 0.f, 0.f};
        const u32 hb0 = WS_HM + (2 * u + (w >> 1)) * HM_TILE + 11 * w * 1024 + lane * 16, hb1 = WS_HM + (2 * u + 1 - (w >> 1)) * HM_TILE + 11 * w * 1024 + lane * 16;
        const bool dead = (w == 3) && (q >= 2);
#pragma unroll
        for (int tt = 0; tt < 2; ++tt) {
          v4u hf[11];
#pragma unroll
          for (int kk = 0; kk < 11; ++kk) hf[kk] = __builtin_amdgcn_raw_buffer_load_b128(rws, (tt ? hb1 : hb0) + kk * 1024, 0, AUX_SC1);
          if (dead) hf[10] = v4u{0u, 0u, 0u, 0u};
#pragma unroll
          for (int kk = 0; kk < 11; ++kk)
#pragma unroll
            for (int t = 0; t < 4; ++t) acc[tt][t] = mfma32(W2[kk][t], hf[kk], acc[tt][t]);
          SB0();
        }
        float res[8];
        reduce(acc, res);
        const f32x4 g0 = *(LAS f32x4*)(L + L_GB + (fcol - n0) * 4), g1 = *(LAS f32x4*)(L + L_GB + (fcol - n0) * 4 + 16);
        const v2u xa = st ? x1s[1][0] : x1s[0][0], xb = st ? x1s[1][1] : x1s[0][1];
        v4u y;
        y.x = pk2(lo16(xa.x) + g0[0] * res[0], hi16(xa.x) + g0[1] * res[1]);
        y.y = pk2(lo16(xa.y) + g0[2] * res[2], hi16(xa.y) + g0[3] * res[3]);
        y.z = pk2(lo16(xb.x) + g1[0] * res[4], hi16(xb.x) + g1[1] * res[5]);
        y.w = pk2(lo16(xb.y) + g1[2] * res[6], hi16(xb.y) + g1[3] * res[7]);
        *(GAS v4u*)(out + (size_t)((2 * u + (w >> 1)) * 16 + tok) * D + fcol) = y;
        if (st == 0) STAMP(13);
      }
    }
    STAMP(15);
  }
}
}

extern "C" void kernel_launch(void* const* d_in, const int* in_sizes, int n_in, void* d_out, int out_size, void* d_ws, size_t ws_size,
                              hipStream_t stream) {
  using mk::u16;
  mk::mega<<<256, 512, 0, stream>>>((const u16*)d_in[0], (const u16*)d_in[1], (const u16*)d_in[2], (const u16*)d_in[4], (const u16*)d_in[5],
                                    (const u16*)d_in[6], (const u16*)d_in[7], (const u16*)d_in[8], (u16*)d_out, (unsigned char*)d_ws);
}
```

```cpp
#include <hip/hip_runtime.h>
#include <stdint.h>

#if defined(__HIP_DEVICE_COMPILE__)
#pragma clang attribute push(__attribute__((target("no-packed-fp32-ops"))), apply_to = function)
#endif
namespace mk {
typedef unsigned short u16;
typedef unsigned u32;
typedef unsigned long long u64;
typedef u32 v4u __attribute__((ext_vector_type(4)));
typedef u32 v2u __attribute__((ext_vector_type(2)));
typedef float f32x4 __attribute__((ext_vector_type(4)));
typedef float f32x2 __attribute__((ext_vector_type(2)));
typedef __bf16 bf16x8 __attribute__((ext_vector_type(8)));
typedef __bf16 bf16x2 __attribute__((ext_vector_type(2)));
typedef short s16x4 __attribute__((ext_vector_type(4)));
#define GAS __attribute__((address_space(1)))
#define LAS __attribute__((address_space(3)))
#define DI __device__ __forceinline__
#define RLX_AGENT __ATOMIC_RELAXED, __HIP_MEMORY_SCOPE_AGENT
#define CBAR() asm volatile("" ::: "memory")
#define DRAIN() asm volatile("s_waitcnt vmcnt(0)" ::: "memory")
#define LGKM0() asm volatile("s_waitcnt lgkmcnt(0)" ::: "memory")
#define SB0() __builtin_amdgcn_sched_barrier(0)
#define PIN(x) asm volatile("" : "+v"(x))

constexpr int S = 2048, D = 512, F = 1376;
constexpr int AUX_SC1 = 16;
constexpr u32 OOB = 0x3ffffff0u;
constexpr u32 WS_EPOCH = 0, WS_ARR = 4096, WS_MOD = 8192, WS_MP = 32768, WS_FLAG = 65536, FSLOT = 256 * 4 * 128;
constexpr u32 WS_O = 2u << 20, WS_X1 = 4u << 20, WS_HM = 6u << 20, WS_ST = 12u << 20, WS_MP2 = (12u << 20) + (512u << 10), WS_END = 13u << 20, WS_DBG = 16u << 20;
constexpr u32 HM_TILE = 177 * 256;
constexpr u32 L_TILE = 16384;
constexpr u32 L_STG = 81920, QSLOT = 24576, WSLOT = 24608, W3OFF = 12320;
constexpr u32 L_SCR2 = 0;
constexpr u32 L_SCR4 = 131072;
constexpr u32 L_MISC = 155776;
constexpr u32 L_MR = L_MISC + 256;
constexpr u32 L_MOD1 = L_MISC + 256;
constexpr u32 L_AS2 = L_MOD1 + 2048;
constexpr u32 L_GA = L_AS2 + 2048;
constexpr u32 L_GB = L_GA + 256;
constexpr u32 L_PART = L_GB + 256;
constexpr u32 L_PF = L_MISC;
constexpr u32 L_CNT = L_PART + 256;
constexpr u32 L_UT = L_CNT + 256;
constexpr u32 L_END = L_UT + 1536;
static_assert(L_END + 1024 <= 163840, "LDS");
enum { C_TAG = 0, C_YBAR = 2, C_XBAR = 3, C_QLOW = 4, C_QALL = 5, C_MOD1 = 6, C_MOD2 = 7, C_H1 = 8  , C_P1DONE = 10, C_PART = 11, C_H2 = 24  , C_RED = 14, C_REDD = 15, C_QL = 16  , C_N = 32 };
constexpr u32 PF_BYTES = 32768;
constexpr u32 SPIN_G = 1u << 14;
constexpr float LOG2E = 1.4426950408889634f;

DI bool wave_all(bool p) { return __builtin_amdgcn_ballot_w64(!p) == 0ull; }
DI u32 pk2(float a, float b) { f32x2 v = {a, b}; return __builtin_bit_cast(u32, __builtin_convertvector(v, bf16x2)); }
DI float lo16(u32 x) { return __builtin_bit_cast(float, x << 16); }
DI float hi16(u32 x) { return __builtin_bit_cast(float, x & 0xffff0000u); }
DI float rbf(float a) { return lo16(pk2(a, 0.f)); }
DI f32x4 mfma32(v4u a, v4u b, f32x4 c) { return __builtin_amdgcn_mfma_f32_16x16x32_bf16(__builtin_bit_cast(bf16x8, a), __builtin_bit_cast(bf16x8, b), c, 0, 0, 0); }
DI float dot2(u32 a, u32 b, float c) { return __builtin_amdgcn_fdot2_f32_bf16(__builtin_bit_cast(bf16x2, a), __builtin_bit_cast(bf16x2, b), c, false); }
template <int CTRL> DI float dpp(float x) { return __builtin_bit_cast(float, __builtin_amdgcn_mov_dpp(__builtin_bit_cast(int, x), CTRL, 0xf, 0xf, true)); }
#define ROR(n) (0x120 + (n))
DI u32 fu(float x) { return __builtin_bit_cast(u32, x); }
DI float uf(u32 x) { return __builtin_bit_cast(float, x); }
DI float x16_add(float x) { auto s = __builtin_amdgcn_permlane16_swap(fu(x), fu(x), false, false); return uf(s[0]) + uf(s[1]); }
DI float x32_add(float x) { auto s = __builtin_amdgcn_permlane32_swap(fu(x), fu(x), false, false); return uf(s[0]) + uf(s[1]); }
DI float fold32(float a, float b) { auto s = __builtin_amdgcn_permlane32_swap(fu(a), fu(b), false, false); return uf(s[0]) + uf(s[1]); }
DI float fold16(float a, float b) { auto s = __builtin_amdgcn_permlane16_swap(fu(a), fu(b), false, false); return uf(s[0]) + uf(s[1]); }
DI int fresh_lane() { int l; asm volatile("v_mbcnt_lo_u32_b32 %0, -1, 0\n\tv_mbcnt_hi_u32_b32 %0, -1, %0" : "=v"(l)); return l; }
#define RELANE() const int lane = fresh_lane(), tok = lane & 15, q = lane >> 4; (void)tok; (void)q

typedef LAS unsigned char* lptr;
typedef __amdgpu_buffer_rsrc_t rsrc_t;
DI v4u lds_r128(lptr L, u32 off) { return *(LAS v4u*)(L + off); }
DI void lds_w128(lptr L, u32 off, v4u v) { *(LAS v4u*)(L + off) = v; }
DI v2u lds_tr(lptr L, u32 off) { return __builtin_bit_cast(v2u, __builtin_amdgcn_ds_read_tr16_b64_v4i16((LAS s16x4*)(L + off))); }
template <int C> DI void sig(u32 cbase) {
  asm volatile("s_mov_b64 exec, 1\n\tds_add_u32 %0, %1 offset:%2\n\ts_mov_b64 exec, -1" ::"v"(cbase), "v"(1u), "n"(4 * C) : "memory");
}
template <int C, int SLP = 1> DI void cwait(u32 cbase, u32 target) {
  u32 t0, t1, tv;
  asm volatile("s_mov_b32 %0, 0x80000\n"
               "1:\n\tds_read_b32 %2, %3 offset:%5\n\ts_waitcnt lgkmcnt(0)\n\tv_readfirstlane_b32 %1, %2\n\ts_cmp_ge_u32 %1, %4\n\ts_cbranch_scc1 2f\n\t"
               "s_sleep %6\n\ts_sub_u32 %0, %0, 1\n\ts_cmp_lg_u32 %0, 0\n\ts_cbranch_scc1 1b\n2:"
               : "=&s"(t0), "=&s"(t1), "=&v"(tv) : "v"(cbase), "s"(target), "n"(4 * C), "n"(SLP) : "memory", "scc");
}
DI v4u mk_rsrc(const void* p, u32 bytes) { const u64 a = (u64)p; return v4u{(u32)a, (u32)(a >> 32), bytes, 0x00020000u}; }
DI void dma_x3(v4u rs, u32 v0, u32 v1, u32 v2, u32 soff, u32 lds) {
  asm volatile("s_mov_b32 m0, %5\n\ts_nop 0\n\tbuffer_load_dwordx4 %0, %3, %4 offen lds\n\tbuffer_load_dwordx4 %1, %3, %4 offen offset:1024 lds\n\t"
               "buffer_load_dwordx4 %2, %3, %4 offen offset:2048 lds" ::"v"(v0), "v"(v1), "v"(v2), "s"(rs), "s"(soff), "s"(lds) : "memory");
}
DI void dma_sc1_x4(v4u rs, u32 v0, u32 soff, u32 lds) {
  asm volatile("s_mov_b32 m0, %3\n\ts_nop 0\n\tbuffer_load_dwordx4 %0, %1, %2 offen sc1 lds\n\tbuffer_load_dwordx4 %0, %1, %2 offen offset:1024 sc1 lds\n\t"
               "buffer_load_dwordx4 %0, %1, %2 offen offset:2048 sc1 lds\n\tbuffer_load_dwordx4 %0, %1, %2 offen offset:3072 sc1 lds" ::"v"(v0), "s"(rs), "s"(soff), "s"(lds) : "memory");
}
DI void dma_sc1_x2(v4u rs, u32 v0, u32 soff, u32 lds) {
  asm volatile("s_mov_b32 m0, %3\n\ts_nop 0\n\tbuffer_load_dwordx4 %0, %1, %2 offen sc1 lds\n\tbuffer_load_dwordx4 %0, %1, %2 offen offset:1024 sc1 lds" ::"v"(v0), "s"(rs), "s"(soff), "s"(lds) : "memory");
}
DI void dma4(const void* sbase, u32 voff, u32 lds) {
  asm volatile("s_mov_b32 m0, %2\n\ts_nop 0\n\tglobal_load_lds_dword %0, %1" ::"v"(voff), "s"(sbase), "s"(lds) : "memory");
}
DI void dma4_sc1(const void* sbase, u32 voff, u32 lds) {
  asm volatile("s_mov_b32 m0, %2\n\ts_nop 0\n\tglobal_load_lds_dword %0, %1 sc1" ::"v"(voff), "s"(sbase), "s"(lds) : "memory");
}
DI void dma16(const void* sbase, u32 voff, u32 lds) {
  asm volatile("s_mov_b32 m0, %2\n\ts_nop 0\n\tglobal_load_lds_dwordx4 %0, %1" ::"v"(voff), "s"(sbase), "s"(lds) : "memory");
}
template <int N> DI void vmwait() { asm volatile("s_waitcnt vmcnt(%0)" ::"n"(N) : "memory"); }

DI u32 flag_off(int slot, int wg, int w) { return WS_FLAG + (u32)(((slot * 256 + wg) * 4 + w) * 128); }
DI void set_flag(v4u rs, u32 off, u32 tag) {
  asm volatile("s_mov_b64 exec, 1\n\tbuffer_store_dword %0, off, %1, %2 sc1\n\ts_mov_b64 exec, -1" ::"v"(tag), "s"(rs), "s"(off) : "memory");
}
DI u32 ld_flag(rsrc_t rs, u32 off) { CBAR(); const u32 f = __builtin_amdgcn_raw_buffer_load_b32(rs, off, 0, AUX_SC1); CBAR(); return f; }
DI void poll_flags(rsrc_t rs, u32 off, u32 tag) {
#pragma unroll 1
  for (u32 i = 0; i < SPIN_G; ++i) {
    const u32 f = ld_flag(rs, off);
    if (wave_all(f == tag)) break;
    __builtin_amdgcn_s_sleep(4);
  }
  __builtin_amdgcn_fence(__ATOMIC_ACQUIRE, "wavefront");
  CBAR();
}
#define WOPS(W) "{v[0:3]}"(W[0][0]), "{v[4:7]}"(W[0][1]), "{v[8:11]}"(W[0][2]), "{v[12:15]}"(W[0][3]), "{v[16:19]}"(W[0][4]), "{v[20:23]}"(W[0][5]), "{v[24:27]}"(W[0][6]), "{v[28:31]}"(W[0][7]), "{v[32:35]}"(W[0][8]), "{v[36:39]}"(W[0][9]), "{v[40:43]}"(W[0][10]), "{v[44:47]}"(W[0][11]), "{v[48:51]}"(W[0][12]), "{v[52:55]}"(W[0][13]), "{v[56:59]}"(W[0][14]), "{v[60:63]}"(W[0][15]), "{v[64:67]}"(W[1][0]), "{v[68:71]}"(W[1][1]), "{v[72:75]}"(W[1][2]), "{v[76:79]}"(W[1][3]), "{v[80:83]}"(W[1][4]), "{v[84:87]}"(W[1][5]), "{v[88:91]}"(W[1][6]), "{v[92:95]}"(W[1][7]), "{v[96:99]}"(W[1][8]), "{v[100:103]}"(W[1][9]), "{v[104:107]}"(W[1][10]), "{v[108:111]}"(W[1][11]), "{v[112:115]}"(W[1][12]), "{v[116:119]}"(W[1][13]), "{v[120:123]}"(W[1][14]), "{v[124:127]}"(W[1][15]), "{v[128:131]}"(W[2][0]), "{v[132:135]}"(W[2][1]), "{v[136:139]}"(W[2][2]), "{v[140:143]}"(W[2][3]), "{v[144:147]}"(W[2][4]), "{v[148:151]}"(W[2][5]), "{v[152:155]}"(W[2][6]), "{v[156:159]}"(W[2][7]), "{v[160:163]}"(W[2][8]), "{v[164:167]}"(W[2][9]), "{v[168:171]}"(W[2][10]), "{v[172:175]}"(W[2][11]), "{v[176:179]}"(W[2][12]), "{v[180:183]}"(W[2][13]), "{v[184:187]}"(W[2][14]), "{v[188:191]}"(W[2][15])
#define CLOB194 "v194", "v195", "v196", "v197", "v198", "v199", "v200", "v201", "v202", "v203", "v204", "v205", "v206", "v207", "v208", "v209", "v210", "v211", "v212", "v213", "v214", "v215", "v216", "v217", "v218", "v219", "v220", "v221", "v222", "v223", "v224", "v225", "v226", "v227", "v228", "v229", "v230", "v231", "v232", "v233", "v234", "v235", "v242", "v243", "v244", "v245", "v246", "v247", "v248", "v249", "v250", "v251", "v252", "v253", "v254", "v255"
#define CLOB_P1 "v196", "v197", "v198", "v199", "v200", "v201", "v202", "v203", "v204", "v205", "v206", "v207", "v208", "v209", "v210", "v211", "v212", "v213", "v214", "v215", "v216", "v217", "v218", "v219", "v220", "v221", "v222", "v223", "v238", "v239", "v240", "v241", "v242", "v243", "v244", "v245", "v246", "v247", "v248", "v249", "v250", "v251", "v252", "v253", "v254", "v255"

__global__ void __launch_bounds__(512, 2)
mega(const u16* __restrict__ x, const u16* __restrict__ wqkv, const u16* __restrict__ wo, const u16* __restrict__ w1, const u16* __restrict__ w3,
     const u16* __restrict__ w2, const u16* __restrict__ cvec, const u16* __restrict__ wmod, u16* __restrict__ out, unsigned char* __restrict__ ws) {
  __shared__ __attribute__((aligned(128))) unsigned char smem[163840];
  lptr L = (lptr)smem;
  const u32 lbase = (u32)(size_t)L, cb = lbase + L_CNT;
  const int wave = __builtin_amdgcn_readfirstlane(__builtin_amdgcn_workitem_id_x() >> 6), w = wave & 3;
  const int bid = __builtin_amdgcn_workgroup_id_x();
  if (bid < 16 && wave * 8192u < PF_BYTES) {
    u64 pc0; asm volatile("s_getpc_b64 %0" : "=s"(pc0));
    const u32 po = __builtin_amdgcn_workitem_id_x() * 128u;
    dma4((const void*)(pc0 & ~127ull), po < PF_BYTES ? po : 0u, lbase + L_PF);
    dma4((const void*)(pc0 & ~127ull), po < PF_BYTES ? po + 64u : 0u, lbase + L_PF);
  }
  asm volatile("" ::"s"(x), "s"(wqkv), "s"(wo), "s"(w1), "s"(w3), "s"(w2), "s"(cvec), "s"(wmod), "s"(out), "s"(ws));
  const int cg1 = (bid & 7) * 8 + ((bid >> 3) & 7), kq1 = bid >> 6;
  if (wave == 0) {
    const u32 l = __builtin_amdgcn_workitem_id_x() & 63u;
    dma4_sc1(ws + WS_EPOCH, 0u, lbase + L_GA);
    dma4(cvec + 128 * kq1, l * 4u, lbase + L_PART);
    const u32 vo = (4u * (l >> 1)) * 6144u + (l & 1u) * 16u;
#pragma unroll
    for (int i = 0; i < 4; ++i) dma16(wmod + (size_t)(128 * kq1 + i) * 3072 + cg1 * 16, vo, lbase + L_MOD1 + i * 1024);
  }
  if (__builtin_amdgcn_workitem_id_x() < C_N) ((LAS u32*)(L + L_CNT))[__builtin_amdgcn_workitem_id_x()] = 0u;
  LGKM0();
  __builtin_amdgcn_s_barrier();
  CBAR();
  const int cl = (bid & 1) * 8 + (bid >> 5), r = ((bid & 7) >> 1) * 4 + ((bid >> 3) & 3), wg = cl * 16 + r;
  const int bl = 2 * cl + (r >> 3);
  const int fs = r & 7;
  const int bl1 = ((bid & 7) >> 1) * 8 + (bid >> 5), hg1 = (bid & 1) * 4 + ((bid >> 3) & 3);
  const int rot = bid >> 5;
  const rsrc_t rws = __builtin_amdgcn_make_buffer_rsrc((void*)ws, 0, (int)WS_END, 0x00020000);
  const v4u rwsv = mk_rsrc(ws, WS_END);
  auto get_tag = [&]() -> u32 {
    cwait<C_TAG + 1>(cb, 1u);
    return __builtin_amdgcn_readfirstlane(*(volatile LAS u32*)(L + L_CNT + 4 * C_TAG));
  };
  auto mod_partial = [&](const v2u cv, const v4u (&wm)[4], float& c0, float& c1) {
    float sc[4];
    { const float c0 = lo16(cv.x), c1 = hi16(cv.x), c2 = lo16(cv.y), c3 = hi16(cv.y);
      sc[0] = rbf(c0 * __builtin_amdgcn_rcpf(1.f + __builtin_amdgcn_exp2f(-LOG2E * c0))); sc[1] = rbf(c1 * __builtin_amdgcn_rcpf(1.f + __builtin_amdgcn_exp2f(-LOG2E * c1)));
      sc[2] = rbf(c2 * __builtin_amdgcn_rcpf(1.f + __builtin_amdgcn_exp2f(-LOG2E * c2))); sc[3] = rbf(c3 * __builtin_amdgcn_rcpf(1.f + __builtin_amdgcn_exp2f(-LOG2E * c3))); }
    float a[8];
#pragma unroll
    for (int j = 0; j < 8; ++j) a[j] = 0.f;
#pragma unroll
    for (int i = 0; i < 4; ++i)
#pragma unroll
      for (int d = 0; d < 4; ++d) { a[2 * d] += sc[i] * lo16(wm[i][d]); a[2 * d + 1] += sc[i] * hi16(wm[i][d]); }
    const float b0 = fold32(a[0], a[4]), b1 = fold32(a[1], a[5]), b2 = fold32(a[2], a[6]), b3 = fold32(a[3], a[7]);
    c0 = fold16(b0, b2); c1 = fold16(b1, b3);
    c0 += dpp<ROR(8)>(c0); c1 += dpp<ROR(8)>(c1);
  };
  auto stage = [&](int g, u32 tag) {
    {
      const int l = fresh_lane();
      poll_flags(rws, flag_off(2, cl * 16 + (w >> 1) * 8 + ((l >> 1) & 7), 2 * g + (l & 1)), tag);
    }
    RELANE();
    const int tile = 8 * cl + 2 * (2 * (w >> 1) + g) + (w & 1);
    const u32 dst = lbase + (g ? 0u : 65536u) + w * L_TILE;
    dma_sc1_x2(rwsv, lane * 16, WS_ST + tile * 2048, lbase + L_SCR4 + g * 8192 + w * 2048);
#pragma unroll
    for (int i = 0; i < 8; ++i) dma_sc1_x2(rwsv, lane * 16, WS_X1 + tile * 16384 + ((i + rot) & 7) * 2048, dst + i * 2048);
    vmwait<16>();
    float sm = 0.f, sq = 0.f;
#pragma unroll
    for (int i = 0; i < 4; ++i) { const f32x2 p = *(LAS f32x2*)(L + L_SCR4 + g * 8192 + w * 2048 + (4 * q + i) * 128 + tok * 8); sm += p.x; sq += p.y; }
    sm = x32_add(x16_add(sm)); sq = x32_add(x16_add(sq));
    const float mean = sm * (1.f / D);
    const float var = __builtin_fmaxf(sq * (1.f / D) - mean * mean, 0.f);
    const float ve = var + 1e-5f, rs = __builtin_amdgcn_rsqf(ve);
    const float val = q == 0 ? -mean : ve * rs;
    const u32 hi = pk2(val, 0.f), lo = pk2(val - lo16(hi), 0.f);
    v2u e = {hi | (lo << 16), hi};
    if (q == 2) e = v2u{fu(rs * LOG2E), fu(ve)};
    if (q < 3) *(LAS v2u*)(L + L_MR + (g * 4 + w) * 384 + lane * 8) = e;
    vmwait<0>();
  };

    const v4u r1 = mk_rsrc(w1, D * F * 2), r3 = mk_rsrc(w3, D * F * 2);
    auto wslot = [&](int ch) -> u32 { return L_STG + (ch % 3) * WSLOT; };

  if (wave < 4) {
    u32 tag = 0;
    {
      RELANE();
      v4u xr[20];
      const rsrc_t rx = __builtin_amdgcn_make_buffer_rsrc((void*)x, 0, S * D * 2, 0x00020000);
      auto xload = [&](int ti) {
        int t = 64 * bl1 - 16 + 16 * ti + 4 * w; t = t < 0 ? 0 : t;
#pragma unroll
        for (int i = 0; i < 4; ++i) xr[ti * 4 + i] = __builtin_amdgcn_raw_buffer_load_b128(rx, lane * 16 + i * 1024, t * 1024, 0);
      };
      xload(0); xload(1);
      SB0();
      dma4(x + (size_t)(64 * bl1 + 16 + 12 * (hg1 & 3) + 3 * w) * D, (lane < 48 ? lane : 0) * 64u, lbase + L_PF);
      f32x2 z[20][4];
      v4u wm2[4]; v2u cv2 = {0u, 0u};
#pragma unroll
      for (int i = 0; i < 4; ++i) wm2[i] = v4u{0u, 0u, 0u, 0u};
      const int lg2 = (w - 1) * 16 + (bid & 15), p2 = bid >> 4;
      v4u g[8];
      auto gload = [&]() {
        CBAR();
#pragma unroll
        for (int i = 0; i < 8; ++i) g[i] = __builtin_amdgcn_raw_buffer_load_b128(rws, lane * 32 + (i & 1) * 16, WS_MP + (i >> 1) * 8192 + w * 2048, AUX_SC1);
        CBAR();
      };
      if (w == 0) vmwait<9>();
      if (w == 0) {
        CBAR();
        const u32 tag = __builtin_amdgcn_readfirstlane(*(LAS u32*)(L + L_GA)) * 1664525u + 1013904223u;
        const v2u cv = *(LAS v2u*)(L + L_PART + (lane >> 1) * 8);
        v4u wm[4];
#pragma unroll
        for (int i = 0; i < 4; ++i) wm[i] = lds_r128(L, L_MOD1 + i * 1024 + lane * 16);
        float c0, c1;
        mod_partial(cv, wm, c0, c1);
        c0 += dpp<ROR(4)>(c0); c1 += dpp<ROR(4)>(c1);
        c0 += dpp<ROR(2)>(c0); c1 += dpp<ROR(2)>(c1);
        if (tok < 4) __hip_atomic_store((GAS u64*)(ws + WS_MP) + kq1 * 1024 + cg1 * 16 + (tok & 1) * 8 + (q & 1) * 2 + (q >> 1) * 4 + (tok >> 1),
                                        ((u64)tag << 32) | fu((tok & 2) ? c1 : c0), RLX_AGENT);
        if (lane == 0) {
          *(LAS v2u*)(L + L_CNT + 4 * C_TAG) = v2u{tag, 1u};
          __hip_atomic_store((GAS u32*)(ws + WS_ARR) + bid, tag, RLX_AGENT);
        }
        CBAR();
      }
      SB0();
#pragma unroll
      for (int ti = 0; ti < 5; ++ti) {
        if (ti < 3) {
#pragma unroll
          for (int i = 0; i < 4; ++i) PIN(xr[ti * 4 + i]);
          xload(ti + 2);
          SB0();
          SB0();
        }
        float sm[4], sq[4];
#pragma unroll
        for (int i = 0; i < 4; ++i) {
          sm[i] = 0.f; sq[i] = 0.f;
#pragma unroll
          for (int d = 0; d < 4; ++d) { sm[i] = dot2(xr[ti * 4 + i][d], 0x3f803f80u, sm[i]); sq[i] = dot2(xr[ti * 4 + i][d], xr[ti * 4 + i][d], sq[i]); }
        }
        float us = fold16(fold32(sm[0], sm[1]), fold32(sm[2], sm[3]));
        float uq = fold16(fold32(sq[0], sq[1]), fold32(sq[2], sq[3]));
        us += dpp<ROR(8)>(us); uq += dpp<ROR(8)>(uq);
        us += dpp<ROR(4)>(us); uq += dpp<ROR(4)>(uq);
        us += dpp<ROR(2)>(us); uq += dpp<ROR(2)>(uq);
        us += dpp<ROR(1)>(us); uq += dpp<ROR(1)>(uq);
        const float mean = us * (1.f / D);
        const float var = __builtin_fmaxf(uq * (1.f / D) - mean * mean, 0.f);
        const float rs = __builtin_amdgcn_rsqf(var + 1e-5f);
        const float nb = -mean * rs;
#pragma unroll
        for (int i = 0; i < 4; ++i) {
          const int ln = (i & 1) * 32 + (i >> 1) * 16;
          const float rsi = uf(__builtin_amdgcn_readlane(fu(rs), ln)), nbi = uf(__builtin_amdgcn_readlane(fu(nb), ln));
#pragma unroll
          for (int d = 0; d < 4; ++d) {
            z[ti * 4 + i][d] = f32x2{lo16(xr[ti * 4 + i][d]), hi16(xr[ti * 4 + i][d])} * rsi + nbi;
            PIN(z[ti * 4 + i][d]);
          }
        }
        SB0();
        if (ti == 3) gload();
        SB0();
      }
      tag = get_tag();
      {
#pragma unroll 1
        for (u32 it = 0; it < SPIN_G; ++it) {
#pragma unroll
          for (int i = 0; i < 8; ++i) PIN(g[i]);
          bool ok = true;
#pragma unroll
          for (int i = 0; i < 8; ++i) ok = ok && g[i].y == tag && g[i].w == tag;
          if (wave_all(ok)) break;
          __builtin_amdgcn_s_sleep(24);
          gload();
        }
        float m4[4];
#pragma unroll
        for (int j = 0; j < 4; ++j) m4[j] = (uf(g[j >> 1][2 * (j & 1)]) + uf(g[2 + (j >> 1)][2 * (j & 1)])) + (uf(g[4 + (j >> 1)][2 * (j & 1)]) + uf(g[6 + (j >> 1)][2 * (j & 1)]));
        u32 p0 = pk2(m4[0], m4[1]), p1 = pk2(m4[2], m4[3]);
        if (w >= 2) { p0 = pk2(1.f + lo16(p0), 1.f + hi16(p0)); p1 = pk2(1.f + lo16(p1), 1.f + hi16(p1)); }
        *(LAS v2u*)(L + L_MOD1 + w * 512 + lane * 8) = v2u{p0, p1};
        sig<C_MOD1>(cb);
      }
      if (w == 1 || w == 2) {
        const int row0 = 32 * p2 + 4 * (lane >> 3);
        cv2 = *(const GAS v2u*)(cvec + row0);
#pragma unroll
        for (int i = 0; i < 4; ++i) wm2[i] = *(const GAS v4u*)(wmod + (size_t)(row0 + i) * 3072 + 1024 + lg2 * 64 + (lane & 7) * 8);
      }
      SB0();
      cwait<C_MOD1>(cb, 4u);
      __builtin_amdgcn_s_setprio(3);
      {
        const v4u sav = lds_r128(L, L_MOD1 + lane * 16), cav = lds_r128(L, L_MOD1 + 1024 + lane * 16);
        f32x2 a1[4], s1[4];
#pragma unroll
        for (int d = 0; d < 4; ++d) { s1[d] = f32x2{lo16(sav[d]), hi16(sav[d])}; a1[d] = f32x2{lo16(cav[d]), hi16(cav[d])}; }
        const u32 wb = ((((lane >> 3) - rot) & 7) * 8 + (lane & 7)) * 256;
        u32 wa[4];
#pragma unroll
        for (int i = 0; i < 4; ++i) wa[i] = wb + (((4 * w + i) ^ (lane & 7)) * 16);
        cwait<0 + C_QLOW>(cb, 4u);
        v4u h[8];
#pragma unroll
        for (int ti = 0; ti < 5; ++ti) {
#pragma unroll
          for (int i = 0; i < 4; ++i) {
            v4u& hh = h[(ti & 1) * 4 + i];
#pragma unroll
            for (int d = 0; d < 4; ++d) { const f32x2 t = z[ti * 4 + i][d] * a1[d] + s1[d]; hh[d] = pk2(t.x, t.y); }
            if (ti < 3) lds_w128(L, ti * L_TILE + wa[i], hh); else PIN(hh);
          }
          if (ti == 1) sig<C_H1>(cb);
          if (ti == 2) sig<C_H1 + 1>(cb);
          SB0();
        }
        cwait<0 + C_QLOW + 16>(cb, 4u);
#pragma unroll
        for (int ti = 3; ti < 5; ++ti)
#pragma unroll
          for (int i = 0; i < 4; ++i) lds_w128(L, ti * L_TILE + wa[i], h[(ti & 1) * 4 + i]);
        sig<C_H1 + 4>(cb);
      }
      if (w == 1 || w == 2) {
        float c0, c1;
        mod_partial(cv2, wm2, c0, c1);
        __hip_atomic_store((GAS u64*)(ws + WS_MP2) + (lg2 * 16 + p2) * 64 + (lane & 7) * 8 + (q & 1) * 2 + (q >> 1) * 4 + ((lane >> 3) & 1),
                           ((u64)tag << 32) | fu((lane & 8) ? c1 : c0), RLX_AGENT);
      }
      __builtin_amdgcn_s_setprio(0);
      {
        const u16* src = w == 0 ? w2 : (w == 1 ? w1 : (w == 2 ? w3 : wo));
        const u32 share = w == 3 ? 2048u : 5504u;
#pragma unroll
        for (int i = 0; i < 6; ++i) {
          const u32 o = i * 1024u + lane * 16u;
          dma16((const unsigned char*)src + (size_t)bid * share, o < share ? o : 0u, lbase + L_UT);
        }
      }
    }
    auto gather2 = [&]() {
      RELANE();
      if (w == 0) {
        v4u g[4]; v2u ga, gb;
#pragma unroll 1
        for (u32 it = 0; it < SPIN_G; ++it) {
          CBAR();
#pragma unroll
          for (int i = 0; i < 4; ++i) g[i] = __builtin_amdgcn_raw_buffer_load_b128(rws, WS_MOD + 768 * 8 + (64 * i + lane) * 16, 0, AUX_SC1);
          ga = __builtin_amdgcn_raw_buffer_load_b64(rws, WS_MOD + (512 + fs * 32 + (lane & 31)) * 8, 0, AUX_SC1);
          gb = __builtin_amdgcn_raw_buffer_load_b64(rws, WS_MOD + (1280 + fs * 32 + (lane & 31)) * 8, 0, AUX_SC1);
          bool ok = ga.y == tag && gb.y == tag;
#pragma unroll
          for (int i = 0; i < 4; ++i) ok = ok && g[i].y == tag && g[i].w == tag;
          if (wave_all(ok)) break;
          __builtin_amdgcn_s_sleep(6);
        }
#pragma unroll
        for (int i = 0; i < 2; ++i) {
          *(LAS v2u*)(L + L_AS2 + 1024 + (64 * i + lane) * 8) = v2u{g[i].x, g[i].z};
          *(LAS v2u*)(L + L_AS2 + (64 * i + lane) * 8) = v2u{pk2(1.f + lo16(g[2 + i].x), 1.f + hi16(g[2 + i].x)), pk2(1.f + lo16(g[2 + i].z), 1.f + hi16(g[2 + i].z))};
        }
        if (lane < 32) { *(LAS f32x2*)(L + L_GA + lane * 8) = f32x2{lo16(ga.x), hi16(ga.x)}; *(LAS f32x2*)(L + L_GB + lane * 8) = f32x2{lo16(gb.x), hi16(gb.x)}; }
        sig<C_MOD2>(cb);
      }
    };
    auto hop2 = [&]() {
      RELANE();
      if (w == 3 && bid < 128) {
        v4u a, b;
#pragma unroll 1
        for (u32 it = 0; it < SPIN_G; ++it) {
          CBAR();
          const u32 o = WS_MP2 + (u32)(((bid >> 2) * 16 + (lane >> 2)) * 512 + (bid & 3) * 128 + (lane & 3) * 32);
          a = __builtin_amdgcn_raw_buffer_load_b128(rws, o, 0, AUX_SC1); b = __builtin_amdgcn_raw_buffer_load_b128(rws, o + 16, 0, AUX_SC1);
          if (wave_all(a.y == tag && a.w == tag && b.y == tag && b.w == tag)) break;
          __builtin_amdgcn_s_sleep(8);
        }
        float s0 = fold32(uf(a.x), uf(b.x)), s1 = fold32(uf(a.z), uf(b.z));
        s0 = x16_add(s0); s1 = x16_add(s1);
        s0 += dpp<ROR(8)>(s0); s1 += dpp<ROR(8)>(s1);
        s0 += dpp<ROR(4)>(s0); s1 += dpp<ROR(4)>(s1);
        if ((lane & 28) == 0) __hip_atomic_store((GAS u64*)(ws + WS_MOD) + 512 + bid * 8 + (lane & 3) * 2 + (lane >> 5), ((u64)tag << 32) | pk2(s0, s1), RLX_AGENT);
      }
      if (w == 0 && bid == 0) {
#pragma unroll 1
        for (u32 it = 0; it < SPIN_G; ++it) {
          CBAR();
          const v4u a = __builtin_amdgcn_raw_buffer_load_b128(rws, WS_ARR + lane * 16, 0, AUX_SC1);
          if (wave_all(a.x == tag && a.y == tag && a.z == tag && a.w == tag)) break;
          __builtin_amdgcn_s_sleep(32);
        }
        if (lane == 0) __hip_atomic_store((GAS u32*)(ws + WS_EPOCH), tag, RLX_AGENT);
      }
    };
    {
      const int v = 4 * r + w;
      v4u W[3][16];
      {
        RELANE();
        u32 tb[3];
#pragma unroll
        for (int t = 0; t < 3; ++t)
          tb[t] = ((lane & 3) >> 1) * W3OFF + (16 * q + ((lane & 15) >> 2)) * 192 + (((3 * w + t) ^ (q & 1)) * 16) + (lane & 1) * 8;
        u32 voff[3];
#pragma unroll
        for (int n = 0; n < 3; ++n) {
          const int Lx = 64 * n + lane, rl = Lx / 12, pp = (Lx - 12 * rl) ^ (w & 1);
          const int col = 96 * r + 8 * pp;
          voff[n] = col < F ? (u32)(((16 * w + rl) * F + col) * 2 - n * 1024) : OOB;
        }
        auto w13_issue = [&](int c0, int c1) {
#pragma unroll
          for (int ch = c0; ch < c1; ++ch) {
            const u32 dst = lbase + wslot(ch) + 16 * w * 192, so = (u32)((ch + rot) & 7) * (64 * F * 2);
            dma_x3(r1, voff[0], voff[1], voff[2], so, dst);
            dma_x3(r3, voff[0], voff[1], voff[2], so, dst + W3OFF);
          }
        };
        cwait<C_QALL>(cb, 4u);
        w13_issue(0, 3);
        vmwait<12>(); sig<C_XBAR>(cb);
        cwait<C_XBAR, 0>(cb, 4u);
        asm volatile("ds_read_b64_tr_b16 v[0:1], %[a0] offset:0\n\tds_read_b64_tr_b16 v[2:3], %[a0] offset:768\n\tds_read_b64_tr_b16 v[64:65], %[a1] offset:0\n\tds_read_b64_tr_b16 v[66:67], %[a1] offset:768\n\tds_read_b64_tr_b16 v[128:129], %[a2] offset:0\n\tds_read_b64_tr_b16 v[130:131], %[a2] offset:768\n\tds_read_b64_tr_b16 v[4:5], %[a0] offset:1536\n\tds_read_b64_tr_b16 v[6:7], %[a0] offset:2304\n\tds_read_b64_tr_b16 v[68:69], %[a1] offset:1536\n\tds_read_b64_tr_b16 v[70:71], %[a1] offset:2304\n\tds_read_b64_tr_b16 v[132:133], %[a2] offset:1536\n\tds_read_b64_tr_b16 v[134:135], %[a2] offset:2304" : "=&{v[0:3]}"(W[0][0]), "=&{v[64:67]}"(W[1][0]), "=&{v[128:131]}"(W[2][0]), "=&{v[4:7]}"(W[0][1]), "=&{v[68:71]}"(W[1][1]), "=&{v[132:135]}"(W[2][1]) : [a0] "v"(lbase + tb[0] + wslot(0)), [a1] "v"(lbase + tb[1] + wslot(0)), [a2] "v"(lbase + tb[2] + wslot(0)) : "memory");
        asm volatile("s_waitcnt lgkmcnt(0)" : "+{v[0:3]}"(W[0][0]), "+{v[64:67]}"(W[1][0]), "+{v[128:131]}"(W[2][0]), "+{v[4:7]}"(W[0][1]), "+{v[68:71]}"(W[1][1]), "+{v[132:135]}"(W[2][1]) :: "memory");
        hop2();
        vmwait<6>(); sig<C_XBAR>(cb);
        cwait<C_XBAR, 0>(cb, 8u);
        asm volatile("ds_read_b64_tr_b16 v[8:9], %[a0] offset:0\n\tds_read_b64_tr_b16 v[10:11], %[a0] offset:768\n\tds_read_b64_tr_b16 v[72:73], %[a1] offset:0\n\tds_read_b64_tr_b16 v[74:75], %[a1] offset:768\n\tds_read_b64_tr_b16 v[136:137], %[a2] offset:0\n\tds_read_b64_tr_b16 v[138:139], %[a2] offset:768\n\tds_read_b64_tr_b16 v[12:13], %[a0] offset:1536\n\tds_read_b64_tr_b16 v[14:15], %[a0] offset:2304\n\tds_read_b64_tr_b16 v[76:77], %[a1] offset:1536\n\tds_read_b64_tr_b16 v[78:79], %[a1] offset:2304\n\tds_read_b64_tr_b16 v[140:141], %[a2] offset:1536\n\tds_read_b64_tr_b16 v[142:143], %[a2] offset:2304" : "=&{v[8:11]}"(W[0][2]), "=&{v[72:75]}"(W[1][2]), "=&{v[136:139]}"(W[2][2]), "=&{v[12:15]}"(W[0][3]), "=&{v[76:79]}"(W[1][3]), "=&{v[140:143]}"(W[2][3]) : [a0] "v"(lbase + tb[0] + wslot(1)), [a1] "v"(lbase + tb[1] + wslot(1)), [a2] "v"(lbase + tb[2] + wslot(1)) : "memory");
        asm volatile("s_waitcnt lgkmcnt(0)" : "+{v[8:11]}"(W[0][2]), "+{v[72:75]}"(W[1][2]), "+{v[136:139]}"(W[2][2]), "+{v[12:15]}"(W[0][3]), "+{v[76:79]}"(W[1][3]), "+{v[140:143]}"(W[2][3]) :: "memory");
        vmwait<0>(); sig<C_XBAR>(cb);
        w13_issue(3, 4);
        cwait<C_XBAR, 0>(cb, 12u);
        asm volatile("ds_read_b64_tr_b16 v[16:17], %[a0] offset:0\n\tds_read_b64_tr_b16 v[18:19], %[a0] offset:768\n\tds_read_b64_tr_b16 v[80:81], %[a1] offset:0\n\tds_read_b64_tr_b16 v[82:83], %[a1] offset:768\n\tds_read_b64_tr_b16 v[144:145], %[a2] offset:0\n\tds_read_b64_tr_b16 v[146:147], %[a2] offset:768\n\tds_read_b64_tr_b16 v[20:21], %[a0] offset:1536\n\tds_read_b64_tr_b16 v[22:23], %[a0] offset:2304\n\tds_read_b64_tr_b16 v[84:85], %[a1] offset:1536\n\tds_read_b64_tr_b16 v[86:87], %[a1] offset:2304\n\tds_read_b64_tr_b16 v[148:149], %[a2] offset:1536\n\tds_read_b64_tr_b16 v[150:151], %[a2] offset:2304" : "=&{v[16:19]}"(W[0][4]), "=&{v[80:83]}"(W[1][4]), "=&{v[144:147]}"(W[2][4]), "=&{v[20:23]}"(W[0][5]), "=&{v[84:87]}"(W[1][5]), "=&{v[148:151]}"(W[2][5]) : [a0] "v"(lbase + tb[0] + wslot(2)), [a1] "v"(lbase + tb[1] + wslot(2)), [a2] "v"(lbase + tb[2] + wslot(2)) : "memory");
        asm volatile("s_waitcnt lgkmcnt(0)" : "+{v[16:19]}"(W[0][4]), "+{v[80:83]}"(W[1][4]), "+{v[144:147]}"(W[2][4]), "+{v[20:23]}"(W[0][5]), "+{v[84:87]}"(W[1][5]), "+{v[148:151]}"(W[2][5]) :: "memory");
        vmwait<0>(); sig<C_XBAR>(cb);
        w13_issue(4, 5);
        cwait<C_XBAR, 0>(cb, 16u);
        asm volatile("ds_read_b64_tr_b16 v[24:25], %[a0] offset:0\n\tds_read_b64_tr_b16 v[26:27], %[a0] offset:768\n\tds_read_b64_tr_b16 v[88:89], %[a1] offset:0\n\tds_read_b64_tr_b16 v[90:91], %[a1] offset:768\n\tds_read_b64_tr_b16 v[152:153], %[a2] offset:0\n\tds_read_b64_tr_b16 v[154:155], %[a2] offset:768\n\tds_read_b64_tr_b16 v[28:29], %[a0] offset:1536\n\tds_read_b64_tr_b16 v[30:31], %[a0] offset:2304\n\tds_read_b64_tr_b16 v[92:93], %[a1] offset:1536\n\tds_read_b64_tr_b16 v[94:95], %[a1] offset:2304\n\tds_read_b64_tr_b16 v[156:157], %[a2] offset:1536\n\tds_read_b64_tr_b16 v[158:159], %[a2] offset:2304" : "=&{v[24:27]}"(W[0][6]), "=&{v[88:91]}"(W[1][6]), "=&{v[152:155]}"(W[2][6]), "=&{v[28:31]}"(W[0][7]), "=&{v[92:95]}"(W[1][7]), "=&{v[156:159]}"(W[2][7]) : [a0] "v"(lbase + tb[0] + wslot(3)), [a1] "v"(lbase + tb[1] + wslot(3)), [a2] "v"(lbase + tb[2] + wslot(3)) : "memory");
        asm volatile("s_waitcnt lgkmcnt(0)" : "+{v[24:27]}"(W[0][6]), "+{v[88:91]}"(W[1][6]), "+{v[152:155]}"(W[2][6]), "+{v[28:31]}"(W[0][7]), "+{v[92:95]}"(W[1][7]), "+{v[156:159]}"(W[2][7]) :: "memory");
        vmwait<0>(); sig<C_XBAR>(cb);
        w13_issue(5, 6);
        cwait<C_XBAR, 0>(cb, 20u);
        asm volatile("ds_read_b64_tr_b16 v[32:33], %[a0] offset:0\n\tds_read_b64_tr_b16 v[34:35], %[a0] offset:768\n\tds_read_b64_tr_b16 v[96:97], %[a1] offset:0\n\tds_read_b64_tr_b16 v[98:99], %[a1] offset:768\n\tds_read_b64_tr_b16 v[160:161], %[a2] offset:0\n\tds_read_b64_tr_b16 v[162:163], %[a2] offset:768\n\tds_read_b64_tr_b16 v[36:37], %[a0] offset:1536\n\tds_read_b64_tr_b16 v[38:39], %[a0] offset:2304\n\tds_read_b64_tr_b16 v[100:101], %[a1] offset:1536\n\tds_read_b64_tr_b16 v[102:103], %[a1] offset:2304\n\tds_read_b64_tr_b16 v[164:165], %[a2] offset:1536\n\tds_read_b64_tr_b16 v[166:167], %[a2] offset:2304" : "=&{v[32:35]}"(W[0][8]), "=&{v[96:99]}"(W[1][8]), "=&{v[160:163]}"(W[2][8]), "=&{v[36:39]}"(W[0][9]), "=&{v[100:103]}"(W[1][9]), "=&{v[164:167]}"(W[2][9]) : [a0] "v"(lbase + tb[0] + wslot(4)), [a1] "v"(lbase + tb[1] + wslot(4)), [a2] "v"(lbase + tb[2] + wslot(4)) : "memory");
        asm volatile("s_waitcnt lgkmcnt(0)" : "+{v[32:35]}"(W[0][8]), "+{v[96:99]}"(W[1][8]), "+{v[160:163]}"(W[2][8]), "+{v[36:39]}"(W[0][9]), "+{v[100:103]}"(W[1][9]), "+{v[164:167]}"(W[2][9]) :: "memory");
        gather2();
        vmwait<0>(); sig<C_XBAR>(cb);
        w13_issue(6, 7);
        cwait<C_XBAR, 0>(cb, 24u);
        asm volatile("ds_read_b64_tr_b16 v[40:41], %[a0] offset:0\n\tds_read_b64_tr_b16 v[42:43], %[a0] offset:768\n\tds_read_b64_tr_b16 v[104:105], %[a1] offset:0\n\tds_read_b64_tr_b16 v[106:107], %[a1] offset:768\n\tds_read_b64_tr_b16 v[168:169], %[a2] offset:0\n\tds_read_b64_tr_b16 v[170:171], %[a2] offset:768\n\tds_read_b64_tr_b16 v[44:45], %[a0] offset:1536\n\tds_read_b64_tr_b16 v[46:47], %[a0] offset:2304\n\tds_read_b64_tr_b16 v[108:109], %[a1] offset:1536\n\tds_read_b64_tr_b16 v[110:111], %[a1] offset:2304\n\tds_read_b64_tr_b16 v[172:173], %[a2] offset:1536\n\tds_read_b64_tr_b16 v[174:175], %[a2] offset:2304" : "=&{v[40:43]}"(W[0][10]), "=&{v[104:107]}"(W[1][10]), "=&{v[168:171]}"(W[2][10]), "=&{v[44:47]}"(W[0][11]), "=&{v[108:111]}"(W[1][11]), "=&{v[172:175]}"(W[2][11]) : [a0] "v"(lbase + tb[0] + wslot(5)), [a1] "v"(lbase + tb[1] + wslot(5)), [a2] "v"(lbase + tb[2] + wslot(5)) : "memory");
        asm volatile("s_waitcnt lgkmcnt(0)" : "+{v[40:43]}"(W[0][10]), "+{v[104:107]}"(W[1][10]), "+{v[168:171]}"(W[2][10]), "+{v[44:47]}"(W[0][11]), "+{v[108:111]}"(W[1][11]), "+{v[172:175]}"(W[2][11]) :: "memory");
        vmwait<0>(); sig<C_XBAR>(cb);
        w13_issue(7, 8);
        cwait<C_XBAR, 0>(cb, 28u);
        asm volatile("ds_read_b64_tr_b16 v[48:49], %[a0] offset:0\n\tds_read_b64_tr_b16 v[50:51], %[a0] offset:768\n\tds_read_b64_tr_b16 v[112:113], %[a1] offset:0\n\tds_read_b64_tr_b16 v[114:115], %[a1] offset:768\n\tds_read_b64_tr_b16 v[176:177], %[a2] offset:0\n\tds_read_b64_tr_b16 v[178:179], %[a2] offset:768\n\tds_read_b64_tr_b16 v[52:53], %[a0] offset:1536\n\tds_read_b64_tr_b16 v[54:55], %[a0] offset:2304\n\tds_read_b64_tr_b16 v[116:117], %[a1] offset:1536\n\tds_read_b64_tr_b16 v[118:119], %[a1] offset:2304\n\tds_read_b64_tr_b16 v[180:181], %[a2] offset:1536\n\tds_read_b64_tr_b16 v[182:183], %[a2] offset:2304" : "=&{v[48:51]}"(W[0][12]), "=&{v[112:115]}"(W[1][12]), "=&{v[176:179]}"(W[2][12]), "=&{v[52:55]}"(W[0][13]), "=&{v[116:119]}"(W[1][13]), "=&{v[180:183]}"(W[2][13]) : [a0] "v"(lbase + tb[0] + wslot(6)), [a1] "v"(lbase + tb[1] + wslot(6)), [a2] "v"(lbase + tb[2] + wslot(6)) : "memory");
        asm volatile("s_waitcnt lgkmcnt(0)" : "+{v[48:51]}"(W[0][12]), "+{v[112:115]}"(W[1][12]), "+{v[176:179]}"(W[2][12]), "+{v[52:55]}"(W[0][13]), "+{v[116:119]}"(W[1][13]), "+{v[180:183]}"(W[2][13]) :: "memory");
        vmwait<0>(); sig<C_XBAR>(cb);
        cwait<C_XBAR, 0>(cb, 32u);
        asm volatile("ds_read_b64_tr_b16 v[56:57], %[a0] offset:0\n\tds_read_b64_tr_b16 v[58:59], %[a0] offset:768\n\tds_read_b64_tr_b16 v[120:121], %[a1] offset:0\n\tds_read_b64_tr_b16 v[122:123], %[a1] offset:768\n\tds_read_b64_tr_b16 v[184:185], %[a2] offset:0\n\tds_read_b64_tr_b16 v[186:187], %[a2] offset:768\n\tds_read_b64_tr_b16 v[60:61], %[a0] offset:1536\n\tds_read_b64_tr_b16 v[62:63], %[a0] offset:2304\n\tds_read_b64_tr_b16 v[124:125], %[a1] offset:1536\n\tds_read_b64_tr_b16 v[126:127], %[a1] offset:2304\n\tds_read_b64_tr_b16 v[188:189], %[a2] offset:1536\n\tds_read_b64_tr_b16 v[190:191], %[a2] offset:2304" : "=&{v[56:59]}"(W[0][14]), "=&{v[120:123]}"(W[1][14]), "=&{v[184:187]}"(W[2][14]), "=&{v[60:63]}"(W[0][15]), "=&{v[124:127]}"(W[1][15]), "=&{v[188:191]}"(W[2][15]) : [a0] "v"(lbase + tb[0] + wslot(7)), [a1] "v"(lbase + tb[1] + wslot(7)), [a2] "v"(lbase + tb[2] + wslot(7)) : "memory");
        asm volatile("s_waitcnt lgkmcnt(0)" : "+{v[56:59]}"(W[0][14]), "+{v[120:123]}"(W[1][14]), "+{v[184:187]}"(W[2][14]), "+{v[60:63]}"(W[0][15]), "+{v[124:127]}"(W[1][15]), "+{v[188:191]}"(W[2][15]) :: "memory");
        sig<C_PART>(cb);
      }
      if (r < 15) {
        cwait<C_MOD2>(cb, 1u);
        v2u aug[3];
        {
          RELANE();
          f32x4 acc[3] = {{0.f, 0.f, 0.f, 0.f}, {0.f, 0.f, 0.f, 0.f}, {0.f, 0.f, 0.f, 0.f}};
          const u32 ab = L_AS2 + (tok >= 4 ? 1024 : 0) + q * 32;
#pragma unroll
          for (int s = 0; s < 16; ++s) {
            const v4u b = lds_r128(L, ab + (((s >> 1) + rot) & 7) * 128 + (s & 1) * 16);
#pragma unroll
            for (int t = 0; t < 3; ++t) acc[t] = mfma32(b, W[t][s], acc[t]);
          }
#pragma unroll
          for (int t = 0; t < 3; ++t) {
            const float val = q < 2 ? acc[t][0] : 0.f;
            const u32 hi = pk2(val, val);
            aug[t] = v2u{hi, pk2(val - lo16(hi), 0.f)};
          }
        }
        cwait<C_PART>(cb, 4u);
        cwait<C_REDD, 2>(cb, 4u);
        stage(0, tag);
        sig<C_H2>(cb); cwait<C_H2>(cb, 4u);
        __builtin_amdgcn_s_setprio(2);
        {
          const bool active = v < 59;
          u32 it, sdelta, foff, lcur, t0, t1; u64 fmask;
          asm volatile(
      "v_mbcnt_lo_u32_b32 v246, -1, 0\n\t"
      "v_mbcnt_hi_u32_b32 v246, -1, v246\n\t"
      "v_lshlrev_b32 v255, 4, v246\n\t"
      "v_add_u32 v255, %[ldsA], v255\n\t"
      "v_and_b32 v194, 31, v246\n\t"
      "v_lshlrev_b32 v194, 3, v194\n\t"
      "v_add_u32 v194, %[mr], v194\n\t"
      "ds_read_b64 v[242:243], v194\n\t"
      "ds_read_b128 v[196:199], v255 offset:0\n\t"
      "ds_read_b128 v[200:203], v255 offset:1024\n\t"
      "ds_read_b128 v[204:207], v255 offset:2048\n\t"
      "ds_read_b128 v[208:211], v255 offset:3072\n\t"
      "v_and_b32 v195, 15, v246\n\t"
      "v_lshlrev_b32 v195, 3, v195\n\t"
      "v_add_u32 v195, %[mr], v195\n\t"
      "v_subrev_u32 v195, 0x80, v195\n\t"
      "s_mov_b32 %[it], 0\n\t"
      "s_mov_b32 %[sdelta], 0x3ffffff0\n\t"
      "s_mov_b64 %[fmask], 0\n\t"
      "s_mov_b32 %[foff], 0\n\t"
      "s_mov_b32 %[lcur], %[ldsA]\n\t"
      "LOOP%=:\n\t"
      "s_cmp_lg_u32 %[it], 2\n\t"
      "s_cbranch_scc1 GO%=\n\t"
      "v_mov_b32 v246, %[cnt]\n\t"
      "v_mov_b32 v247, 1\n\t"
      "s_mov_b64 exec, 1\n\t"
      "ds_add_u32 v246, v247 offset:4\n\t"
      "s_mov_b64 exec, -1\n\t"
      "s_mov_b32 %[t0], 0x80000\n\t"
      "SPIN%=:\n\t"
      "ds_read_b32 v247, v246\n\t"
      "s_waitcnt lgkmcnt(0)\n\t"
      "v_readfirstlane_b32 %[t1], v247\n\t"
      "s_cmp_ge_u32 %[t1], 4\n\t"
      "s_cbranch_scc1 RDY%=\n\t"
      "s_sleep 1\n\t"
      "s_sub_u32 %[t0], %[t0], 1\n\t"
      "s_cmp_lg_u32 %[t0], 0\n\t"
      "s_cbranch_scc1 SPIN%=\n\t"
      "RDY%=:\n\t"
      "s_sub_u32 %[t0], %[ldsB], %[lcur]\n\t"
      "v_add_u32 v255, %[t0], v255\n\t"
      "s_sub_u32 %[sdelta], %[sdelta], %[t0]\n\t"
      "s_mov_b32 %[lcur], %[ldsB]\n\t"
      "ds_read_b64 v[242:243], v194\n\t"
      "ds_read_b128 v[196:199], v255 offset:0\n\t"
      "ds_read_b128 v[200:203], v255 offset:1024\n\t"
      "ds_read_b128 v[204:207], v255 offset:2048\n\t"
      "ds_read_b128 v[208:211], v255 offset:3072\n\t"
      "GO%=:\n\t"
      "s_waitcnt lgkmcnt(4)\n\t"
      "v_mfma_f32_16x16x16_bf16 v[212:215], v[236:237], v[242:243], 0\n\t"
      "ds_read_b64 v[244:245], v195 offset:0\n\t"
      "v_mfma_f32_16x16x16_bf16 v[216:219], v[238:239], v[242:243], 0\n\t"
      "v_permlane32_swap_b32 v224, v226\n\t"
      "v_mfma_f32_16x16x16_bf16 v[220:223], v[240:241], v[242:243], 0\n\t"
      "v_add_u32 v194, 0x180, v194\n\t"
      "ds_read_b64 v[242:243], v194 offset:0\n\t"
      "s_waitcnt lgkmcnt(4)\n\t"
      "v_mfma_f32_16x16x32_bf16 v[212:215], v[0:3], v[196:199], v[212:215]\n\t"
      "v_permlane32_swap_b32 v225, v227\n\t"
      "v_mfma_f32_16x16x32_bf16 v[216:219], v[64:67], v[196:199], v[216:219]\n\t"
      "v_mul_f32 v226, v224, v226\n\t"
      "v_mul_f32 v227, v225, v227\n\t"
      "v_mfma_f32_16x16x32_bf16 v[220:223], v[128:131], v[196:199], v[220:223]\n\t"
      "ds_read_b128 v[196:199], v255 offset:4096\n\t"
      "s_waitcnt lgkmcnt(2)\n\t"
      "v_mul_f32 v224, v224, v244\n\t"
      "v_mfma_f32_16x16x32_bf16 v[212:215], v[4:7], v[200:203], v[212:215]\n\t"
      "v_mul_f32 v225, v225, v244\n\t"
      "v_mfma_f32_16x16x32_bf16 v[216:219], v[68:71], v[200:203], v[216:219]\n\t"
      "v_exp_f32 v224, -v224\n\t"
      "v_mfma_f32_16x16x32_bf16 v[220:223], v[132:135], v[200:203], v[220:223]\n\t"
      "ds_read_b128 v[200:203], v255 offset:5120\n\t"
      "v_fma_f32 v224, v224, v245, v245\n\t"
      "v_mfma_f32_16x16x32_bf16 v[212:215], v[8:11], v[204:207], v[212:215]\n\t"
      "v_exp_f32 v225, -v225\n\t"
      "v_mfma_f32_16x16x32_bf16 v[216:219], v[72:75], v[204:207], v[216:219]\n\t"
      "v_fma_f32 v225, v225, v245, v245\n\t"
      "v_mfma_f32_16x16x32_bf16 v[220:223], v[136:139], v[204:207], v[220:223]\n\t"
      "ds_read_b128 v[204:207], v255 offset:6144\n\t"
      "v_mfma_f32_16x16x32_bf16 v[212:215], v[12:15], v[208:211], v[212:215]\n\t"
      "v_rcp_f32 v224, v224\n\t"
      "v_mfma_f32_16x16x32_bf16 v[216:219], v[76:79], v[208:211], v[216:219]\n\t"
      "v_rcp_f32 v225, v225\n\t"
      "v_mfma_f32_16x16x32_bf16 v[220:223], v[140:143], v[208:211], v[220:223]\n\t"
      "ds_read_b128 v[208:211], v255 offset:7168\n\t"
      "v_mul_f32 v224, v224, v226\n\t"
      "s_waitcnt lgkmcnt(2)\n\t"
      "v_mfma_f32_16x16x32_bf16 v[212:215], v[16:19], v[196:199], v[212:215]\n\t"
      "v_mul_f32 v225, v225, v227\n\t"
      "v_cvt_pk_bf16_f32 v248, v224, v225\n\t"
      "v_mfma_f32_16x16x32_bf16 v[216:219], v[80:83], v[196:199], v[216:219]\n\t"
      "v_cvt_pk_bf16_f32 v252, v224, v225\n\t"
      "v_add_u32 v246, %[sdelta], v255\n\t"
      "v_mfma_f32_16x16x32_bf16 v[220:223], v[144:147], v[196:199], v[220:223]\n\t"
      "ds_read_b128 v[196:199], v255 offset:8192\n\t"
      "v_add_u32 v195, 0x180, v195\n\t"
      "v_mfma_f32_16x16x32_bf16 v[212:215], v[20:23], v[200:203], v[212:215]\n\t"
      "v_permlane32_swap_b32 v228, v230\n\t"
      "v_mfma_f32_16x16x32_bf16 v[216:219], v[84:87], v[200:203], v[216:219]\n\t"
      "v_permlane32_swap_b32 v229, v231\n\t"
      "v_mfma_f32_16x16x32_bf16 v[220:223], v[148:151], v[200:203], v[220:223]\n\t"
      "ds_read_b128 v[200:203], v255 offset:9216\n\t"
      "v_mul_f32 v230, v228, v230\n\t"
      "s_waitcnt lgkmcnt(2)\n\t"
      "v_mfma_f32_16x16x32_bf16 v[212:215], v[24:27], v[204:207], v[212:215]\n\t"
      "v_mul_f32 v231, v229, v231\n\t"
      "v_mul_f32 v228, v228, v244\n\t"
      "v_mfma_f32_16x16x32_bf16 v[216:219], v[88:91], v[204:207], v[216:219]\n\t"
      "v_mul_f32 v229, v229, v244\n\t"
      "v_mfma_f32_16x16x32_bf16 v[220:223], v[152:155], v[204:207], v[220:223]\n\t"
      "ds_read_b128 v[204:207], v255 offset:10240\n\t"
      "v_mfma_f32_16x16x32_bf16 v[212:215], v[28:31], v[208:211], v[212:215]\n\t"
      "v_exp_f32 v228, -v228\n\t"
      "v_mfma_f32_16x16x32_bf16 v[216:219], v[92:95], v[208:211], v[216:219]\n\t"
      "v_exp_f32 v229, -v229\n\t"
      "v_fma_f32 v228, v228, v245, v245\n\t"
      "v_mfma_f32_16x16x32_bf16 v[220:223], v[156:159], v[208:211], v[220:223]\n\t"
      "ds_read_b128 v[208:211], v255 offset:11264\n\t"
      "v_fma_f32 v229, v229, v245, v245\n\t"
      "s_waitcnt lgkmcnt(2)\n\t"
      "v_mfma_f32_16x16x32_bf16 v[212:215], v[32:35], v[196:199], v[212:215]\n\t"
      "v_rcp_f32 v228, v228\n\t"
      "v_mfma_f32_16x16x32_bf16 v[216:219], v[96:99], v[196:199], v[216:219]\n\t"
      "v_rcp_f32 v229, v229\n\t"
      "v_mfma_f32_16x16x32_bf16 v[220:223], v[160:163], v[196:199], v[220:223]\n\t"
      "ds_read_b128 v[196:199], v255 offset:12288\n\t"
      "v_mul_f32 v228, v228, v230\n\t"
      "v_mfma_f32_16x16x32_bf16 v[212:215], v[36:39], v[200:203], v[212:215]\n\t"
      "v_mul_f32 v229, v229, v231\n\t"
      "v_cvt_pk_bf16_f32 v249, v228, v229\n\t"
      "v_mfma_f32_16x16x32_bf16 v[216:219], v[100:103], v[200:203], v[216:219]\n\t"
      "v_cvt_pk_bf16_f32 v253, v228, v229\n\t"
      "v_cvt_pk_bf16_f32 v251, v228, v229\n\t"
      "v_mfma_f32_16x16x32_bf16 v[220:223], v[164:167], v[200:203], v[220:223]\n\t"
      "ds_read_b128 v[200:203], v255 offset:13312\n\t"
      "s_waitcnt lgkmcnt(2)\n\t"
      "v_mfma_f32_16x16x32_bf16 v[212:215], v[40:43], v[204:207], v[212:215]\n\t"
      "v_permlane32_swap_b32 v253, v251\n\t"
      "v_mfma_f32_16x16x32_bf16 v[216:219], v[104:107], v[204:207], v[216:219]\n\t"
      "v_permlane32_swap_b32 v232, v234\n\t"
      "v_mul_f32 v234, v232, v234\n\t"
      "v_mfma_f32_16x16x32_bf16 v[220:223], v[168:171], v[204:207], v[220:223]\n\t"
      "ds_read_b128 v[204:207], v255 offset:14336\n\t"
      "v_mul_f32 v232, v232, v244\n\t"
      "v_mfma_f32_16x16x32_bf16 v[212:215], v[44:47], v[208:211], v[212:215]\n\t"
      "v_permlane32_swap_b32 v233, v235\n\t"
      "v_mfma_f32_16x16x32_bf16 v[216:219], v[108:111], v[208:211], v[216:219]\n\t"
      "v_mul_f32 v235, v233, v235\n\t"
      "v_mul_f32 v233, v233, v244\n\t"
      "v_mfma_f32_16x16x32_bf16 v[220:223], v[172:175], v[208:211], v[220:223]\n\t"
      "ds_read_b128 v[208:211], v255 offset:15360\n\t"
      "s_waitcnt lgkmcnt(2)\n\t"
      "v_mfma_f32_16x16x32_bf16 v[212:215], v[48:51], v[196:199], v[212:215]\n\t"
      "v_exp_f32 v232, -v232\n\t"
      "v_mfma_f32_16x16x32_bf16 v[216:219], v[112:115], v[196:199], v[216:219]\n\t"
      "v_exp_f32 v233, -v233\n\t"
      "v_fma_f32 v232, v232, v245, v245\n\t"
      "v_mfma_f32_16x16x32_bf16 v[220:223], v[176:179], v[196:199], v[220:223]\n\t"
      "ds_read_b128 v[196:199], v255 offset:16384\n\t"
      "v_fma_f32 v233, v233, v245, v245\n\t"
      "v_mfma_f32_16x16x32_bf16 v[212:215], v[52:55], v[200:203], v[212:215]\n\t"
      "v_rcp_f32 v232, v232\n\t"
      "v_mfma_f32_16x16x32_bf16 v[216:219], v[116:119], v[200:203], v[216:219]\n\t"
      "v_rcp_f32 v233, v233\n\t"
      "v_mfma_f32_16x16x32_bf16 v[220:223], v[180:183], v[200:203], v[220:223]\n\t"
      "ds_read_b128 v[200:203], v255 offset:17408\n\t"
      "v_mul_f32 v232, v232, v234\n\t"
      "s_waitcnt lgkmcnt(2)\n\t"
      "v_mfma_f32_16x16x32_bf16 v[212:215], v[56:59], v[204:207], v[212:215]\n\t"
      "v_mul_f32 v233, v233, v235\n\t"
      "v_cvt_pk_bf16_f32 v250, v232, v233\n\t"
      "v_mfma_f32_16x16x32_bf16 v[216:219], v[120:123], v[204:207], v[216:219]\n\t"
      "v_cvt_pk_bf16_f32 v254, v232, v233\n\t"
      "v_mfma_f32_16x16x32_bf16 v[220:223], v[184:187], v[204:207], v[220:223]\n\t"
      "ds_read_b128 v[204:207], v255 offset:18432\n\t"
      "v_permlane32_swap_b32 v252, v254\n\t"
      "v_mfma_f32_16x16x32_bf16 v[212:215], v[60:63], v[208:211], v[212:215]\n\t"
      "v_mfma_f32_16x16x32_bf16 v[216:219], v[124:127], v[208:211], v[216:219]\n\t"
      "v_permlane16_swap_b32 v254, v251\n\t"
      "v_mfma_f32_16x16x32_bf16 v[220:223], v[188:191], v[208:211], v[220:223]\n\t"
      "ds_read_b128 v[208:211], v255 offset:19456\n\t"
      "s_mov_b64 exec, %[m48]\n\tbuffer_store_dwordx4 v[248:251], v246, %[rs], 0 offen sc1\n\ts_mov_b64 exec, -1\n\t"
      "v_add_u32 v255, 0x4000, v255\n\t"
      "v_mfma_f32_16x16x16_bf16 v[224:227], v[236:237], v[242:243], 0\n\t"
      "s_and_b32 %[t0], %[it], 1\n\t"
      "s_lshl_b32 %[t0], %[t0], 1\n\t"
      "s_lshr_b32 %[t1], %[it], 1\n\t"
      "v_mfma_f32_16x16x16_bf16 v[228:231], v[238:239], v[242:243], 0\n\t"
      "s_or_b32 %[t0], %[t0], %[t1]\n\t"
      "s_mul_i32 %[t1], %[t0], 0x16200\n\t"
      "s_add_u32 %[t1], %[t1], %[hm]\n\t"
      "v_mfma_f32_16x16x16_bf16 v[232:235], v[240:241], v[242:243], 0\n\t"
      "v_add_u32 v194, 0x180, v194\n\t"
      "ds_read_b64 v[242:243], v194 offset:0\n\t"
      "s_sub_u32 %[sdelta], %[t1], %[lcur]\n\t"
      "s_waitcnt lgkmcnt(3)\n\t"
      "v_mfma_f32_16x16x32_bf16 v[224:227], v[0:3], v[196:199], v[224:227]\n\t"
      "s_sub_u32 %[sdelta], %[sdelta], 0x4000\n\t"
      "s_add_u32 %[lcur], %[lcur], 0x8000\n\t"
      "s_lshl_b32 %[t0], %[t0], 17\n\t"
      "v_mfma_f32_16x16x32_bf16 v[228:231], v[64:67], v[196:199], v[228:231]\n\t"
      "s_add_u32 %[it], %[it], 1\n\t"
      "ds_read_b64 v[244:245], v195 offset:0\n\t"
      "v_mfma_f32_16x16x32_bf16 v[232:235], v[128:131], v[196:199], v[232:235]\n\t"
      "ds_read_b128 v[196:199], v255 offset:4096\n\t"
      "v_permlane32_swap_b32 v212, v214\n\t"
      "v_mfma_f32_16x16x32_bf16 v[224:227], v[4:7], v[200:203], v[224:227]\n\t"
      "v_permlane32_swap_b32 v213, v215\n\t"
      "v_mul_f32 v214, v212, v214\n\t"
      "v_mfma_f32_16x16x32_bf16 v[228:231], v[68:71], v[200:203], v[228:231]\n\t"
      "v_mul_f32 v215, v213, v215\n\t"
      "s_waitcnt lgkmcnt(1)\n\t"
      "v_mul_f32 v212, v212, v244\n\t"
      "v_mul_f32 v213, v213, v244\n\t"
      "v_mfma_f32_16x16x32_bf16 v[232:235], v[132:135], v[200:203], v[232:235]\n\t"
      "ds_read_b128 v[200:203], v255 offset:5120\n\t"
      "v_exp_f32 v212, -v212\n\t"
      "v_mfma_f32_16x16x32_bf16 v[224:227], v[8:11], v[204:207], v[224:227]\n\t"
      "v_exp_f32 v213, -v213\n\t"
      "v_fma_f32 v212, v212, v245, v245\n\t"
      "v_mfma_f32_16x16x32_bf16 v[228:231], v[72:75], v[204:207], v[228:231]\n\t"
      "v_fma_f32 v213, v213, v245, v245\n\t"
      "v_rcp_f32 v212, v212\n\t"
      "v_mfma_f32_16x16x32_bf16 v[232:235], v[136:139], v[204:207], v[232:235]\n\t"
      "ds_read_b128 v[204:207], v255 offset:6144\n\t"
      "v_mul_f32 v212, v212, v214\n\t"
      "v_mfma_f32_16x16x32_bf16 v[224:227], v[12:15], v[208:211], v[224:227]\n\t"
      "v_rcp_f32 v213, v213\n\t"
      "v_mfma_f32_16x16x32_bf16 v[228:231], v[76:79], v[208:211], v[228:231]\n\t"
      "v_mul_f32 v213, v213, v215\n\t"
      "v_cvt_pk_bf16_f32 v248, v212, v213\n\t"
      "v_mfma_f32_16x16x32_bf16 v[232:235], v[140:143], v[208:211], v[232:235]\n\t"
      "ds_read_b128 v[208:211], v255 offset:7168\n\t"
      "v_cvt_pk_bf16_f32 v252, v212, v213\n\t"
      "s_waitcnt lgkmcnt(2)\n\t"
      "v_mfma_f32_16x16x32_bf16 v[224:227], v[16:19], v[196:199], v[224:227]\n\t"
      "v_permlane32_swap_b32 v216, v218\n\t"
      "v_mfma_f32_16x16x32_bf16 v[228:231], v[80:83], v[196:199], v[228:231]\n\t"
      "v_permlane32_swap_b32 v217, v219\n\t"
      "v_mfma_f32_16x16x32_bf16 v[232:235], v[144:147], v[196:199], v[232:235]\n\t"
      "ds_read_b128 v[196:199], v255 offset:8192\n\t"
      "v_mul_f32 v218, v216, v218\n\t"
      "v_mfma_f32_16x16x32_bf16 v[224:227], v[20:23], v[200:203], v[224:227]\n\t"
      "v_mul_f32 v219, v217, v219\n\t"
      "v_mul_f32 v216, v216, v244\n\t"
      "v_mfma_f32_16x16x32_bf16 v[228:231], v[84:87], v[200:203], v[228:231]\n\t"
      "v_mul_f32 v217, v217, v244\n\t"
      "v_add_u32 v246, %[sdelta], v255\n\t"
      "v_mfma_f32_16x16x32_bf16 v[232:235], v[148:151], v[200:203], v[232:235]\n\t"
      "ds_read_b128 v[200:203], v255 offset:9216\n\t"
      "v_mov_b32 v247, %[tag]\n\t"
      "s_waitcnt lgkmcnt(2)\n\t"
      "v_mfma_f32_16x16x32_bf16 v[224:227], v[24:27], v[204:207], v[224:227]\n\t"
      "v_exp_f32 v216, -v216\n\t"
      "v_mfma_f32_16x16x32_bf16 v[228:231], v[88:91], v[204:207], v[228:231]\n\t"
      "v_exp_f32 v217, -v217\n\t"
      "v_mfma_f32_16x16x32_bf16 v[232:235], v[152:155], v[204:207], v[232:235]\n\t"
      "ds_read_b128 v[204:207], v255 offset:10240\n\t"
      "v_fma_f32 v216, v216, v245, v245\n\t"
      "v_mfma_f32_16x16x32_bf16 v[224:227], v[28:31], v[208:211], v[224:227]\n\t"
      "v_fma_f32 v217, v217, v245, v245\n\t"
      "v_add_u32 v195, 0x180, v195\n\t"
      "v_mfma_f32_16x16x32_bf16 v[228:231], v[92:95], v[208:211], v[228:231]\n\t"
      "v_rcp_f32 v216, v216\n\t"
      "v_mfma_f32_16x16x32_bf16 v[232:235], v[156:159], v[208:211], v[232:235]\n\t"
      "ds_read_b128 v[208:211], v255 offset:11264\n\t"
      "v_mul_f32 v216, v216, v218\n\t"
      "s_waitcnt lgkmcnt(2)\n\t"
      "v_mfma_f32_16x16x32_bf16 v[224:227], v[32:35], v[196:199], v[224:227]\n\t"
      "v_rcp_f32 v217, v217\n\t"
      "v_mfma_f32_16x16x32_bf16 v[228:231], v[96:99], v[196:199], v[228:231]\n\t"
      "v_mul_f32 v217, v217, v219\n\t"
      "v_cvt_pk_bf16_f32 v249, v216, v217\n\t"
      "v_mfma_f32_16x16x32_bf16 v[232:235], v[160:163], v[196:199], v[232:235]\n\t"
      "ds_read_b128 v[196:199], v255 offset:12288\n\t"
      "v_cvt_pk_bf16_f32 v253, v216, v217\n\t"
      "v_mfma_f32_16x16x32_bf16 v[224:227], v[36:39], v[200:203], v[224:227]\n\t"
      "v_cvt_pk_bf16_f32 v251, v216, v217\n\t"
      "v_mfma_f32_16x16x32_bf16 v[228:231], v[100:103], v[200:203], v[228:231]\n\t"
      "v_permlane32_swap_b32 v220, v222\n\t"
      "v_mul_f32 v222, v220, v222\n\t"
      "v_mfma_f32_16x16x32_bf16 v[232:235], v[164:167], v[200:203], v[232:235]\n\t"
      "ds_read_b128 v[200:203], v255 offset:13312\n\t"
      "v_mul_f32 v220, v220, v244\n\t"
      "s_waitcnt lgkmcnt(2)\n\t"
      "v_mfma_f32_16x16x32_bf16 v[224:227], v[40:43], v[204:207], v[224:227]\n\t"
      "v_permlane32_swap_b32 v253, v251\n\t"
      "v_mfma_f32_16x16x32_bf16 v[228:231], v[104:107], v[204:207], v[228:231]\n\t"
      "v_permlane32_swap_b32 v221, v223\n\t"
      "v_mfma_f32_16x16x32_bf16 v[232:235], v[168:171], v[204:207], v[232:235]\n\t"
      "ds_read_b128 v[204:207], v255 offset:14336\n\t"
      "v_mul_f32 v223, v221, v223\n\t"
      "v_mfma_f32_16x16x32_bf16 v[224:227], v[44:47], v[208:211], v[224:227]\n\t"
      "v_mul_f32 v221, v221, v244\n\t"
      "v_mfma_f32_16x16x32_bf16 v[228:231], v[108:111], v[208:211], v[228:231]\n\t"
      "v_exp_f32 v220, -v220\n\t"
      "v_mfma_f32_16x16x32_bf16 v[232:235], v[172:175], v[208:211], v[232:235]\n\t"
      "ds_read_b128 v[208:211], v255 offset:15360\n\t"
      "v_exp_f32 v221, -v221\n\t"
      "s_waitcnt lgkmcnt(2)\n\t"
      "v_mfma_f32_16x16x32_bf16 v[224:227], v[48:51], v[196:199], v[224:227]\n\t"
      "v_fma_f32 v220, v220, v245, v245\n\t"
      "v_fma_f32 v221, v221, v245, v245\n\t"
      "v_mfma_f32_16x16x32_bf16 v[228:231], v[112:115], v[196:199], v[228:231]\n\t"
      "v_rcp_f32 v220, v220\n\t"
      "v_mfma_f32_16x16x32_bf16 v[232:235], v[176:179], v[196:199], v[232:235]\n\t"
      "ds_read_b128 v[196:199], v255 offset:16384\n\t"
      "v_mul_f32 v220, v220, v222\n\t"
      "v_mfma_f32_16x16x32_bf16 v[224:227], v[52:55], v[200:203], v[224:227]\n\t"
      "v_rcp_f32 v221, v221\n\t"
      "v_mfma_f32_16x16x32_bf16 v[228:231], v[116:119], v[200:203], v[228:231]\n\t"
      "v_mul_f32 v221, v221, v223\n\t"
      "v_cvt_pk_bf16_f32 v250, v220, v221\n\t"
      "v_mfma_f32_16x16x32_bf16 v[232:235], v[180:183], v[200:203], v[232:235]\n\t"
      "ds_read_b128 v[200:203], v255 offset:17408\n\t"
      "v_cvt_pk_bf16_f32 v254, v220, v221\n\t"
      "s_waitcnt lgkmcnt(2)\n\t"
      "v_mfma_f32_16x16x32_bf16 v[224:227], v[56:59], v[204:207], v[224:227]\n\t"
      "v_permlane32_swap_b32 v252, v254\n\t"
      "v_mfma_f32_16x16x32_bf16 v[228:231], v[120:123], v[204:207], v[228:231]\n\t"
      "v_mfma_f32_16x16x32_bf16 v[232:235], v[184:187], v[204:207], v[232:235]\n\t"
      "ds_read_b128 v[204:207], v255 offset:18432\n\t"
      "v_permlane16_swap_b32 v254, v251\n\t"
      "v_mfma_f32_16x16x32_bf16 v[224:227], v[60:63], v[208:211], v[224:227]\n\t"
      "s_waitcnt vmcnt(0)\n\ts_mov_b64 exec, %[fmask]\n\tbuffer_store_dword v247, off, %[rs], %[foff] sc1\n\ts_mov_b64 exec, %[m48]\n\tbuffer_store_dwordx4 v[248:251], v246, %[rs], 0 offen sc1\n\ts_mov_b64 exec, -1\n\t"
      "v_mfma_f32_16x16x32_bf16 v[228:231], v[124:127], v[208:211], v[228:231]\n\t"
      "v_mfma_f32_16x16x32_bf16 v[232:235], v[188:191], v[208:211], v[232:235]\n\t"
      "ds_read_b128 v[208:211], v255 offset:19456\n\t"
      "v_add_u32 v255, 0x4000, v255\n\t"
      "s_add_u32 %[sdelta], %[sdelta], 0x7100\n\t"
      "s_add_u32 %[foff], %[t0], %[fl]\n\t"
      "s_cmp_lg_u32 %[act], 0\n\t"
      "s_cselect_b64 %[fmask], 1, 0\n\t"
      "s_cmp_lt_u32 %[it], 4\n\t"
      "s_cbranch_scc1 LOOP%=\n\t"
      "ds_read_b64 v[244:245], v195 offset:0\n\t"
      "v_permlane32_swap_b32 v224, v226\n\t"
      "v_permlane32_swap_b32 v225, v227\n\t"
      "v_mul_f32 v226, v224, v226\n\t"
      "v_mul_f32 v227, v225, v227\n\t"
      "s_waitcnt lgkmcnt(0)\n\t"
      "v_mul_f32 v224, v224, v244\n\t"
      "v_mul_f32 v225, v225, v244\n\t"
      "v_exp_f32 v224, -v224\n\t"
      "v_exp_f32 v225, -v225\n\t"
      "v_fma_f32 v224, v224, v245, v245\n\t"
      "v_fma_f32 v225, v225, v245, v245\n\t"
      "v_rcp_f32 v224, v224\n\t"
      "v_rcp_f32 v225, v225\n\t"
      "v_mul_f32 v224, v224, v226\n\t"
      "v_mul_f32 v225, v225, v227\n\t"
      "v_cvt_pk_bf16_f32 v248, v224, v225\n\t"
      "v_cvt_pk_bf16_f32 v252, v224, v225\n\t"
      "v_permlane32_swap_b32 v228, v230\n\t"
      "v_permlane32_swap_b32 v229, v231\n\t"
      "v_mul_f32 v230, v228, v230\n\t"
      "v_mul_f32 v231, v229, v231\n\t"
      "v_mul_f32 v228, v228, v244\n\t"
      "v_mul_f32 v229, v229, v244\n\t"
      "v_exp_f32 v228, -v228\n\t"
      "v_exp_f32 v229, -v229\n\t"
      "v_fma_f32 v228, v228, v245, v245\n\t"
      "v_fma_f32 v229, v229, v245, v245\n\t"
      "v_rcp_f32 v228, v228\n\t"
      "v_rcp_f32 v229, v229\n\t"
      "v_mul_f32 v228, v228, v230\n\t"
      "v_mul_f32 v229, v229, v231\n\t"
      "v_cvt_pk_bf16_f32 v249, v228, v229\n\t"
      "v_cvt_pk_bf16_f32 v253, v228, v229\n\t"
      "v_cvt_pk_bf16_f32 v251, v228, v229\n\t"
      "s_nop 1\n\t"
      "v_permlane32_swap_b32 v253, v251\n\t"
      "v_permlane32_swap_b32 v232, v234\n\t"
      "v_permlane32_swap_b32 v233, v235\n\t"
      "v_mul_f32 v234, v232, v234\n\t"
      "v_mul_f32 v235, v233, v235\n\t"
      "v_mul_f32 v232, v232, v244\n\t"
      "v_mul_f32 v233, v233, v244\n\t"
      "v_exp_f32 v232, -v232\n\t"
      "v_exp_f32 v233, -v233\n\t"
      "v_fma_f32 v232, v232, v245, v245\n\t"
      "v_fma_f32 v233, v233, v245, v245\n\t"
      "v_rcp_f32 v232, v232\n\t"
      "v_rcp_f32 v233, v233\n\t"
      "v_mul_f32 v232, v232, v234\n\t"
      "v_mul_f32 v233, v233, v235\n\t"
      "v_cvt_pk_bf16_f32 v250, v232, v233\n\t"
      "v_cvt_pk_bf16_f32 v254, v232, v233\n\t"
      "v_add_u32 v246, %[sdelta], v255\n\t"
      "s_nop 0\n\t"
      "v_permlane32_swap_b32 v252, v254\n\t"
      "v_add_u32 v195, 0x180, v195\n\t"
      "s_nop 0\n\t"
      "v_permlane16_swap_b32 v254, v251\n\t"
      "s_mov_b64 exec, %[m48]\n\tbuffer_store_dwordx4 v[248:251], v246, %[rs], 0 offen sc1\n\ts_mov_b64 exec, -1\n\t"
      "v_mov_b32 v247, %[tag]\n\t"
      "s_waitcnt vmcnt(0) lgkmcnt(0)\n\t"
      "s_mov_b64 exec, %[fmask]\n\t"
      "buffer_store_dword v247, off, %[rs], %[foff] sc1\n\t"
      "s_mov_b64 exec, -1\n\t"
              : [it] "=&s"(it), [sdelta] "=&s"(sdelta), [foff] "=&s"(foff), [lcur] "=&s"(lcur), [t0] "=&s"(t0), [t1] "=&s"(t1), [fmask] "=&s"(fmask)
              : WOPS(W), [rs] "s"(rwsv), [tag] "s"(tag), [ldsA] "s"(lbase + 65536u), [ldsB] "s"(lbase), "{v[236:237]}"(aug[0]), "{v[238:239]}"(aug[1]), "{v[240:241]}"(aug[2]), [mr] "s"(lbase + L_MR),
                [cnt] "s"(cb + 4 * (C_H2 + 1)), [hm] "s"(__builtin_amdgcn_readfirstlane(active ? WS_HM + 8 * cl * HM_TILE + v * 768 : OOB)), [fl] "s"(flag_off(4, wg, w)),
                [act] "s"(__builtin_amdgcn_readfirstlane(active ? 1 : 0)), [m48] "s"(0x0000ffffffffffffull)
              : "memory", "scc", CLOB194);
        }
        sig<27>(cb);
      }
    }
  } else {
    u32 tag;
    {
      v4u W[3][16];
      {
        RELANE();
        const v4u rq = mk_rsrc(wqkv, D * 3 * D * 2);
        const int pos = lane & 7, gs = ((lane >> 4) & 1) | ((w & 1) << 1);
        const u32 voff = (u32)(((8 * w + (lane >> 3)) * 1536 + 64 * hg1 + ((pos >> 1) ^ gs) * 16 + (pos & 1) * 8) * 2);
        auto slot = [&](int ch) -> u32 { return ch < 2 ? L_STG + ch * QSLOT : (ch < 4 ? (ch - 2) * QSLOT : (ch == 4 ? L_STG + 2 * QSLOT : (ch == 5 ? 2 * QSLOT : L_STG + (ch - 6) * QSLOT))); };
        auto issue = [&](int ch) {
          const u32 so = (u32)((ch + rot) & 7) * (64 * 3072);
          if (ch) vmwait<12>();
          dma_x3(rq, voff, voff, voff, so, lbase + slot(ch) + w * 3072);
          if (ch) vmwait<12>();
          dma_x3(rq, voff, voff, voff, so + 32 * 3072, lbase + slot(ch) + (w + 4) * 3072);
        };
        issue(0);
        vmwait<0>();
        issue(1); issue(2); issue(3);
        const int i4 = (lane & 15) >> 2;
        const u32 tb = q * 3072 + i4 * 128 + ((w ^ ((i4 >> 1) | ((q & 1) << 1))) * 32) + (lane & 3) * 8;
        vmwait<12>(); sig<C_QL>(cb);
        issue(4); issue(5);
        vmwait<12>(); sig<C_QL + 1>(cb);
        cwait<0 + C_QL>(cb, 4u);
        asm volatile("ds_read_b64_tr_b16 v[0:1], %[a0] offset:0\n\tds_read_b64_tr_b16 v[2:3], %[a0] offset:512\n\tds_read_b64_tr_b16 v[64:65], %[a0] offset:1024\n\tds_read_b64_tr_b16 v[66:67], %[a0] offset:1536\n\tds_read_b64_tr_b16 v[128:129], %[a0] offset:2048\n\tds_read_b64_tr_b16 v[130:131], %[a0] offset:2560\n\tds_read_b64_tr_b16 v[4:5], %[a0] offset:12288\n\tds_read_b64_tr_b16 v[6:7], %[a0] offset:12800\n\tds_read_b64_tr_b16 v[68:69], %[a0] offset:13312\n\tds_read_b64_tr_b16 v[70:71], %[a0] offset:13824\n\tds_read_b64_tr_b16 v[132:133], %[a0] offset:14336\n\tds_read_b64_tr_b16 v[134:135], %[a0] offset:14848" : "=&{v[0:3]}"(W[0][0]), "=&{v[64:67]}"(W[1][0]), "=&{v[128:131]}"(W[2][0]), "=&{v[4:7]}"(W[0][1]), "=&{v[68:71]}"(W[1][1]), "=&{v[132:135]}"(W[2][1]) : [a0] "v"(lbase + tb + slot(0)) : "memory");
        asm volatile("ds_read_b64_tr_b16 v[8:9], %[a0] offset:0\n\tds_read_b64_tr_b16 v[10:11], %[a0] offset:512\n\tds_read_b64_tr_b16 v[72:73], %[a0] offset:1024\n\tds_read_b64_tr_b16 v[74:75], %[a0] offset:1536\n\tds_read_b64_tr_b16 v[136:137], %[a0] offset:2048\n\tds_read_b64_tr_b16 v[138:139], %[a0] offset:2560\n\tds_read_b64_tr_b16 v[12:13], %[a0] offset:12288\n\tds_read_b64_tr_b16 v[14:15], %[a0] offset:12800\n\tds_read_b64_tr_b16 v[76:77], %[a0] offset:13312\n\tds_read_b64_tr_b16 v[78:79], %[a0] offset:13824\n\tds_read_b64_tr_b16 v[140:141], %[a0] offset:14336\n\tds_read_b64_tr_b16 v[142:143], %[a0] offset:14848" : "=&{v[8:11]}"(W[0][2]), "=&{v[72:75]}"(W[1][2]), "=&{v[136:139]}"(W[2][2]), "=&{v[12:15]}"(W[0][3]), "=&{v[76:79]}"(W[1][3]), "=&{v[140:143]}"(W[2][3]) : [a0] "v"(lbase + tb + slot(1)) : "memory");
        asm volatile("s_waitcnt lgkmcnt(0)" : "+{v[0:3]}"(W[0][0]), "+{v[64:67]}"(W[1][0]), "+{v[128:131]}"(W[2][0]), "+{v[4:7]}"(W[0][1]), "+{v[68:71]}"(W[1][1]), "+{v[132:135]}"(W[2][1]), "+{v[8:11]}"(W[0][2]), "+{v[72:75]}"(W[1][2]), "+{v[136:139]}"(W[2][2]), "+{v[12:15]}"(W[0][3]), "+{v[76:79]}"(W[1][3]), "+{v[140:143]}"(W[2][3]) :: "memory");
        sig<C_YBAR>(cb);
        cwait<0 + C_QL + 1>(cb, 4u);
        asm volatile("ds_read_b64_tr_b16 v[16:17], %[a0] offset:0\n\tds_read_b64_tr_b16 v[18:19], %[a0] offset:512\n\tds_read_b64_tr_b16 v[80:81], %[a0] offset:1024\n\tds_read_b64_tr_b16 v[82:83], %[a0] offset:1536\n\tds_read_b64_tr_b16 v[144:145], %[a0] offset:2048\n\tds_read_b64_tr_b16 v[146:147], %[a0] offset:2560\n\tds_read_b64_tr_b16 v[20:21], %[a0] offset:12288\n\tds_read_b64_tr_b16 v[22:23], %[a0] offset:12800\n\tds_read_b64_tr_b16 v[84:85], %[a0] offset:13312\n\tds_read_b64_tr_b16 v[86:87], %[a0] offset:13824\n\tds_read_b64_tr_b16 v[148:149], %[a0] offset:14336\n\tds_read_b64_tr_b16 v[150:151], %[a0] offset:14848" : "=&{v[16:19]}"(W[0][4]), "=&{v[80:83]}"(W[1][4]), "=&{v[144:147]}"(W[2][4]), "=&{v[20:23]}"(W[0][5]), "=&{v[84:87]}"(W[1][5]), "=&{v[148:151]}"(W[2][5]) : [a0] "v"(lbase + tb + slot(2)) : "memory");
        asm volatile("ds_read_b64_tr_b16 v[24:25], %[a0] offset:0\n\tds_read_b64_tr_b16 v[26:27], %[a0] offset:512\n\tds_read_b64_tr_b16 v[88:89], %[a0] offset:1024\n\tds_read_b64_tr_b16 v[90:91], %[a0] offset:1536\n\tds_read_b64_tr_b16 v[152:153], %[a0] offset:2048\n\tds_read_b64_tr_b16 v[154:155], %[a0] offset:2560\n\tds_read_b64_tr_b16 v[28:29], %[a0] offset:12288\n\tds_read_b64_tr_b16 v[30:31], %[a0] offset:12800\n\tds_read_b64_tr_b16 v[92:93], %[a0] offset:13312\n\tds_read_b64_tr_b16 v[94:95], %[a0] offset:13824\n\tds_read_b64_tr_b16 v[156:157], %[a0] offset:14336\n\tds_read_b64_tr_b16 v[158:159], %[a0] offset:14848" : "=&{v[24:27]}"(W[0][6]), "=&{v[88:91]}"(W[1][6]), "=&{v[152:155]}"(W[2][6]), "=&{v[28:31]}"(W[0][7]), "=&{v[92:95]}"(W[1][7]), "=&{v[156:159]}"(W[2][7]) : [a0] "v"(lbase + tb + slot(3)) : "memory");
        asm volatile("s_waitcnt lgkmcnt(0)" : "+{v[16:19]}"(W[0][4]), "+{v[80:83]}"(W[1][4]), "+{v[144:147]}"(W[2][4]), "+{v[20:23]}"(W[0][5]), "+{v[84:87]}"(W[1][5]), "+{v[148:151]}"(W[2][5]), "+{v[24:27]}"(W[0][6]), "+{v[88:91]}"(W[1][6]), "+{v[152:155]}"(W[2][6]), "+{v[28:31]}"(W[0][7]), "+{v[92:95]}"(W[1][7]), "+{v[156:159]}"(W[2][7]) :: "memory");
        sig<C_QLOW>(cb);
        cwait<C_YBAR>(cb, 4u);
        issue(6); issue(7);
        vmwait<12>(); sig<C_QL + 2>(cb);
        cwait<0 + C_QL + 2>(cb, 4u);
        asm volatile("ds_read_b64_tr_b16 v[32:33], %[a0] offset:0\n\tds_read_b64_tr_b16 v[34:35], %[a0] offset:512\n\tds_read_b64_tr_b16 v[96:97], %[a0] offset:1024\n\tds_read_b64_tr_b16 v[98:99], %[a0] offset:1536\n\tds_read_b64_tr_b16 v[160:161], %[a0] offset:2048\n\tds_read_b64_tr_b16 v[162:163], %[a0] offset:2560\n\tds_read_b64_tr_b16 v[36:37], %[a0] offset:12288\n\tds_read_b64_tr_b16 v[38:39], %[a0] offset:12800\n\tds_read_b64_tr_b16 v[100:101], %[a0] offset:13312\n\tds_read_b64_tr_b16 v[102:103], %[a0] offset:13824\n\tds_read_b64_tr_b16 v[164:165], %[a0] offset:14336\n\tds_read_b64_tr_b16 v[166:167], %[a0] offset:14848" : "=&{v[32:35]}"(W[0][8]), "=&{v[96:99]}"(W[1][8]), "=&{v[160:163]}"(W[2][8]), "=&{v[36:39]}"(W[0][9]), "=&{v[100:103]}"(W[1][9]), "=&{v[164:167]}"(W[2][9]) : [a0] "v"(lbase + tb + slot(4)) : "memory");
        asm volatile("ds_read_b64_tr_b16 v[40:41], %[a0] offset:0\n\tds_read_b64_tr_b16 v[42:43], %[a0] offset:512\n\tds_read_b64_tr_b16 v[104:105], %[a0] offset:1024\n\tds_read_b64_tr_b16 v[106:107], %[a0] offset:1536\n\tds_read_b64_tr_b16 v[168:169], %[a0] offset:2048\n\tds_read_b64_tr_b16 v[170:171], %[a0] offset:2560\n\tds_read_b64_tr_b16 v[44:45], %[a0] offset:12288\n\tds_read_b64_tr_b16 v[46:47], %[a0] offset:12800\n\tds_read_b64_tr_b16 v[108:109], %[a0] offset:13312\n\tds_read_b64_tr_b16 v[110:111], %[a0] offset:13824\n\tds_read_b64_tr_b16 v[172:173], %[a0] offset:14336\n\tds_read_b64_tr_b16 v[174:175], %[a0] offset:14848" : "=&{v[40:43]}"(W[0][10]), "=&{v[104:107]}"(W[1][10]), "=&{v[168:171]}"(W[2][10]), "=&{v[44:47]}"(W[0][11]), "=&{v[108:111]}"(W[1][11]), "=&{v[172:175]}"(W[2][11]) : [a0] "v"(lbase + tb + slot(5)) : "memory");
        asm volatile("s_waitcnt lgkmcnt(0)" : "+{v[32:35]}"(W[0][8]), "+{v[96:99]}"(W[1][8]), "+{v[160:163]}"(W[2][8]), "+{v[36:39]}"(W[0][9]), "+{v[100:103]}"(W[1][9]), "+{v[164:167]}"(W[2][9]), "+{v[40:43]}"(W[0][10]), "+{v[104:107]}"(W[1][10]), "+{v[168:171]}"(W[2][10]), "+{v[44:47]}"(W[0][11]), "+{v[108:111]}"(W[1][11]), "+{v[172:175]}"(W[2][11]) :: "memory");
        sig<C_QLOW + 16>(cb);
        vmwait<0>(); sig<C_QL + 3>(cb);
        cwait<0 + C_QL + 3>(cb, 4u);
        asm volatile("ds_read_b64_tr_b16 v[48:49], %[a0] offset:0\n\tds_read_b64_tr_b16 v[50:51], %[a0] offset:512\n\tds_read_b64_tr_b16 v[112:113], %[a0] offset:1024\n\tds_read_b64_tr_b16 v[114:115], %[a0] offset:1536\n\tds_read_b64_tr_b16 v[176:177], %[a0] offset:2048\n\tds_read_b64_tr_b16 v[178:179], %[a0] offset:2560\n\tds_read_b64_tr_b16 v[52:53], %[a0] offset:12288\n\tds_read_b64_tr_b16 v[54:55], %[a0] offset:12800\n\tds_read_b64_tr_b16 v[116:117], %[a0] offset:13312\n\tds_read_b64_tr_b16 v[118:119], %[a0] offset:13824\n\tds_read_b64_tr_b16 v[180:181], %[a0] offset:14336\n\tds_read_b64_tr_b16 v[182:183], %[a0] offset:14848" : "=&{v[48:51]}"(W[0][12]), "=&{v[112:115]}"(W[1][12]), "=&{v[176:179]}"(W[2][12]), "=&{v[52:55]}"(W[0][13]), "=&{v[116:119]}"(W[1][13]), "=&{v[180:183]}"(W[2][13]) : [a0] "v"(lbase + tb + slot(6)) : "memory");
        asm volatile("ds_read_b64_tr_b16 v[56:57], %[a0] offset:0\n\tds_read_b64_tr_b16 v[58:59], %[a0] offset:512\n\tds_read_b64_tr_b16 v[120:121], %[a0] offset:1024\n\tds_read_b64_tr_b16 v[122:123], %[a0] offset:1536\n\tds_read_b64_tr_b16 v[184:185], %[a0] offset:2048\n\tds_read_b64_tr_b16 v[186:187], %[a0] offset:2560\n\tds_read_b64_tr_b16 v[60:61], %[a0] offset:12288\n\tds_read_b64_tr_b16 v[62:63], %[a0] offset:12800\n\tds_read_b64_tr_b16 v[124:125], %[a0] offset:13312\n\tds_read_b64_tr_b16 v[126:127], %[a0] offset:13824\n\tds_read_b64_tr_b16 v[188:189], %[a0] offset:14336\n\tds_read_b64_tr_b16 v[190:191], %[a0] offset:14848" : "=&{v[56:59]}"(W[0][14]), "=&{v[120:123]}"(W[1][14]), "=&{v[184:187]}"(W[2][14]), "=&{v[60:63]}"(W[0][15]), "=&{v[124:127]}"(W[1][15]), "=&{v[188:191]}"(W[2][15]) : [a0] "v"(lbase + tb + slot(7)) : "memory");
        asm volatile("s_waitcnt lgkmcnt(0)" : "+{v[48:51]}"(W[0][12]), "+{v[112:115]}"(W[1][12]), "+{v[176:179]}"(W[2][12]), "+{v[52:55]}"(W[0][13]), "+{v[116:119]}"(W[1][13]), "+{v[180:183]}"(W[2][13]), "+{v[56:59]}"(W[0][14]), "+{v[120:123]}"(W[1][14]), "+{v[184:187]}"(W[2][14]), "+{v[60:63]}"(W[0][15]), "+{v[124:127]}"(W[1][15]), "+{v[188:191]}"(W[2][15]) :: "memory");
        sig<C_QALL>(cb);
      }
      tag = get_tag();
      const u32 obase = WS_O + ((2 * bl1) * 32 + 4 * hg1 + w) * 1024;
      v2u qc, k0, k1, misc; v4u vv, od; u64 par, c0, c1;
      __builtin_amdgcn_s_setprio(2);
      {
        u32 it, so, bias, t0, t1; u64 fmask;
        asm volatile(
      "v_mbcnt_lo_u32_b32 v253, -1, 0\n\t"
      "v_mbcnt_hi_u32_b32 v253, -1, v253\n\t"
      "v_lshlrev_b32 v195, 4, v253\n\t"
      "v_and_b32 v250, 15, v253\n\t"
      "v_lshrrev_b32 v251, 4, v253\n\t"
      "v_xor_b32 v254, v250, v251\n\t"
      "v_lshlrev_b32 v254, 4, v254\n\t"
      "v_lshl_add_u32 v254, v251, 8, v254\n\t"
      "v_add_u32 v254, %[lds], v254\n\t"
      "v_xor_b32 v255, 64, v254\n\t"
      "v_and_b32 v252, 1, v250\n\t"
      "v_cmp_eq_u32 %[par], 1, v252\n\t"
      "v_lshl_add_u32 v252, v251, 2, v252\n\t"
      "v_cmp_le_u32 %[c0], v252, v250\n\t"
      "v_add_u32 v252, 2, v252\n\t"
      "v_cmp_le_u32 %[c1], v252, v250\n\t"
      "v_mov_b32 v194, 0xffff\n\t"
      "v_mov_b32 v252, 0xffff0000\n\t"
      "v_cndmask_b32 v194, v194, v252, %[par]\n\t"
      "v_mov_b32 v224, 0\n\t"
      "v_mov_b32 v225, 0\n\t"
      "v_mov_b32 v228, 0\n\t"
      "v_mov_b32 v229, 0\n\t"
      "v_mov_b32 v232, 0\n\t"
      "v_mov_b32 v233, 0\n\t"
      "s_mov_b32 %[it], 0\n\t"
      "s_mov_b32 %[so], 0x3ffffff0\n\t"
      "s_mov_b64 %[fmask], 0\n\t"
      "s_mov_b32 %[bias], 0\n\t"
      "v_mov_b32 v253, %[cnt]\n\t"
      "s_mov_b32 %[t0], 0x80000\n\t"
      "WA%=:\n\t"
      "ds_read_b32 v252, v253 offset:0\n\t"
      "s_waitcnt lgkmcnt(0)\n\t"
      "v_readfirstlane_b32 %[t1], v252\n\t"
      "s_cmp_ge_u32 %[t1], 4\n\t"
      "s_cbranch_scc1 WAD%=\n\t"
      "s_sleep 1\n\t"
      "s_sub_u32 %[t0], %[t0], 1\n\t"
      "s_cmp_lg_u32 %[t0], 0\n\t"
      "s_cbranch_scc1 WA%=\n\t"
      "WAD%=:\n\t"
      "ds_read_b128 v[196:199], v254 offset:0\n\t"
      "ds_read_b128 v[200:203], v255 offset:1024\n\t"
      "ds_read_b128 v[204:207], v254 offset:2048\n\t"
      "ds_read_b128 v[208:211], v255 offset:3072\n\t"
      "s_waitcnt lgkmcnt(2)\n\t"
      "v_mfma_f32_16x16x32_bf16 v[216:219], v[64:67], v[196:199], 0\n\t"
      "v_mfma_f32_16x16x32_bf16 v[220:223], v[196:199], v[128:131], 0\n\t"
      "ds_read_b128 v[196:199], v254 offset:4096\n\t"
      "v_mfma_f32_16x16x32_bf16 v[216:219], v[68:71], v[200:203], v[216:219]\n\t"
      "v_mfma_f32_16x16x32_bf16 v[220:223], v[200:203], v[132:135], v[220:223]\n\t"
      "ds_read_b128 v[200:203], v255 offset:5120\n\t"
      "s_waitcnt lgkmcnt(2)\n\t"
      "v_mfma_f32_16x16x32_bf16 v[216:219], v[72:75], v[204:207], v[216:219]\n\t"
      "v_mfma_f32_16x16x32_bf16 v[220:223], v[204:207], v[136:139], v[220:223]\n\t"
      "ds_read_b128 v[204:207], v254 offset:6144\n\t"
      "v_mfma_f32_16x16x32_bf16 v[216:219], v[76:79], v[208:211], v[216:219]\n\t"
      "v_mfma_f32_16x16x32_bf16 v[220:223], v[208:211], v[140:143], v[220:223]\n\t"
      "ds_read_b128 v[208:211], v255 offset:7168\n\t"
      "s_waitcnt lgkmcnt(2)\n\t"
      "v_mfma_f32_16x16x32_bf16 v[216:219], v[80:83], v[196:199], v[216:219]\n\t"
      "v_mfma_f32_16x16x32_bf16 v[220:223], v[196:199], v[144:147], v[220:223]\n\t"
      "ds_read_b128 v[196:199], v254 offset:8192\n\t"
      "v_mfma_f32_16x16x32_bf16 v[216:219], v[84:87], v[200:203], v[216:219]\n\t"
      "v_mfma_f32_16x16x32_bf16 v[220:223], v[200:203], v[148:151], v[220:223]\n\t"
      "ds_read_b128 v[200:203], v255 offset:9216\n\t"
      "s_waitcnt lgkmcnt(2)\n\t"
      "v_mfma_f32_16x16x32_bf16 v[216:219], v[88:91], v[204:207], v[216:219]\n\t"
      "v_mfma_f32_16x16x32_bf16 v[220:223], v[204:207], v[152:155], v[220:223]\n\t"
      "ds_read_b128 v[204:207], v254 offset:10240\n\t"
      "v_mfma_f32_16x16x32_bf16 v[216:219], v[92:95], v[208:211], v[216:219]\n\t"
      "v_mfma_f32_16x16x32_bf16 v[220:223], v[208:211], v[156:159], v[220:223]\n\t"
      "ds_read_b128 v[208:211], v255 offset:11264\n\t"
      "s_waitcnt lgkmcnt(2)\n\t"
      "v_mfma_f32_16x16x32_bf16 v[216:219], v[96:99], v[196:199], v[216:219]\n\t"
      "v_mfma_f32_16x16x32_bf16 v[220:223], v[196:199], v[160:163], v[220:223]\n\t"
      "ds_read_b128 v[196:199], v254 offset:12288\n\t"
      "v_mfma_f32_16x16x32_bf16 v[216:219], v[100:103], v[200:203], v[216:219]\n\t"
      "v_mfma_f32_16x16x32_bf16 v[220:223], v[200:203], v[164:167], v[220:223]\n\t"
      "ds_read_b128 v[200:203], v255 offset:13312\n\t"
      "s_waitcnt lgkmcnt(2)\n\t"
      "v_mfma_f32_16x16x32_bf16 v[216:219], v[104:107], v[204:207], v[216:219]\n\t"
      "v_mfma_f32_16x16x32_bf16 v[220:223], v[204:207], v[168:171], v[220:223]\n\t"
      "ds_read_b128 v[204:207], v254 offset:14336\n\t"
      "v_mfma_f32_16x16x32_bf16 v[216:219], v[108:111], v[208:211], v[216:219]\n\t"
      "v_mfma_f32_16x16x32_bf16 v[220:223], v[208:211], v[172:175], v[220:223]\n\t"
      "ds_read_b128 v[208:211], v255 offset:15360\n\t"
      "s_waitcnt lgkmcnt(2)\n\t"
      "v_mfma_f32_16x16x32_bf16 v[216:219], v[112:115], v[196:199], v[216:219]\n\t"
      "v_mfma_f32_16x16x32_bf16 v[220:223], v[196:199], v[176:179], v[220:223]\n\t"
      "ds_read_b128 v[196:199], v254 offset:16384\n\t"
      "v_mfma_f32_16x16x32_bf16 v[216:219], v[116:119], v[200:203], v[216:219]\n\t"
      "v_mfma_f32_16x16x32_bf16 v[220:223], v[200:203], v[180:183], v[220:223]\n\t"
      "ds_read_b128 v[200:203], v255 offset:17408\n\t"
      "s_waitcnt lgkmcnt(2)\n\t"
      "v_mfma_f32_16x16x32_bf16 v[216:219], v[120:123], v[204:207], v[216:219]\n\t"
      "v_mfma_f32_16x16x32_bf16 v[220:223], v[204:207], v[184:187], v[220:223]\n\t"
      "ds_read_b128 v[204:207], v254 offset:18432\n\t"
      "v_mfma_f32_16x16x32_bf16 v[216:219], v[124:127], v[208:211], v[216:219]\n\t"
      "v_mfma_f32_16x16x32_bf16 v[220:223], v[208:211], v[188:191], v[220:223]\n\t"
      "ds_read_b128 v[208:211], v255 offset:19456\n\t"
      "v_add_u32 v254, 0x4000, v254\n\t"
      "v_add_u32 v255, 0x4000, v255\n\t"
      "s_nop 3\n\t"
      "v_cvt_pk_bf16_f32 v226, v216, v217\n\t"
      "v_cvt_pk_bf16_f32 v227, v218, v219\n\t"
      "v_cvt_pk_bf16_f32 v230, v220, v221\n\t"
      "v_cvt_pk_bf16_f32 v231, v222, v223\n\t"
      "v_mov_b32 v253, %[cnt]\n\t"
      "s_mov_b32 %[t0], 0x80000\n\t"
      "WB%=:\n\t"
      "ds_read_b32 v252, v253 offset:4\n\t"
      "s_waitcnt lgkmcnt(0)\n\t"
      "v_readfirstlane_b32 %[t1], v252\n\t"
      "s_cmp_ge_u32 %[t1], 4\n\t"
      "s_cbranch_scc1 WBD%=\n\t"
      "s_sleep 1\n\t"
      "s_sub_u32 %[t0], %[t0], 1\n\t"
      "s_cmp_lg_u32 %[t0], 0\n\t"
      "s_cbranch_scc1 WB%=\n\t"
      "WBD%=:\n\t"
      "LOOP%=:\n\t"
      "s_waitcnt lgkmcnt(2)\n\t"
      "v_mfma_f32_16x16x32_bf16 v[212:215], v[0:3], v[196:199], 0\n\t"
      "v_mfma_f32_16x16x16_bf16 v[238:241], v[226:227], v[224:225], 0\n\t"
      "v_mfma_f32_16x16x32_bf16 v[216:219], v[64:67], v[196:199], 0\n\t"
      "v_mfma_f32_16x16x16_bf16 v[242:245], v[228:229], v[224:225], 0\n\t"
      "v_mfma_f32_16x16x32_bf16 v[220:223], v[196:199], v[128:131], 0\n\t"
      "ds_read_b128 v[196:199], v254 offset:4096\n\t"
      "v_mfma_f32_16x16x32_bf16 v[212:215], v[4:7], v[200:203], v[212:215]\n\t"
      "v_mfma_f32_16x16x32_bf16 v[216:219], v[68:71], v[200:203], v[216:219]\n\t"
      "v_mfma_f32_16x16x32_bf16 v[220:223], v[200:203], v[132:135], v[220:223]\n\t"
      "ds_read_b128 v[200:203], v255 offset:5120\n\t"
      "v_cndmask_b32 v250, v238, v239, %[par]\n\t"
      "s_waitcnt lgkmcnt(2)\n\t"
      "v_mfma_f32_16x16x32_bf16 v[212:215], v[8:11], v[204:207], v[212:215]\n\t"
      "v_cndmask_b32 v251, v240, v241, %[par]\n\t"
      "v_cndmask_b32 v242, v242, v243, %[par]\n\t"
      "v_mfma_f32_16x16x32_bf16 v[216:219], v[72:75], v[204:207], v[216:219]\n\t"
      "v_cndmask_b32 v244, v244, v245, %[par]\n\t"
      "v_add_f32 v242, %[bias], v242\n\t"
      "v_mfma_f32_16x16x32_bf16 v[220:223], v[204:207], v[136:139], v[220:223]\n\t"
      "ds_read_b128 v[204:207], v254 offset:6144\n\t"
      "v_add_f32 v244, %[bias], v244\n\t"
      "v_mfma_f32_16x16x32_bf16 v[212:215], v[12:15], v[208:211], v[212:215]\n\t"
      "v_cndmask_b32 v250, v242, v250, %[c0]\n\t"
      "v_cndmask_b32 v251, v244, v251, %[c1]\n\t"
      "v_mfma_f32_16x16x32_bf16 v[216:219], v[76:79], v[208:211], v[216:219]\n\t"
      "v_max_f32 v252, v250, v251\n\t"
      "v_max_f32 v253, v250, v251\n\t"
      "v_mfma_f32_16x16x32_bf16 v[220:223], v[208:211], v[140:143], v[220:223]\n\t"
      "ds_read_b128 v[208:211], v255 offset:7168\n\t"
      "s_waitcnt lgkmcnt(2)\n\t"
      "v_mfma_f32_16x16x32_bf16 v[212:215], v[16:19], v[196:199], v[212:215]\n\t"
      "v_permlane16_swap_b32 v252, v253\n\t"
      "v_mfma_f32_16x16x32_bf16 v[216:219], v[80:83], v[196:199], v[216:219]\n\t"
      "v_max_f32 v242, v252, v253\n\t"
      "v_max_f32 v243, v252, v253\n\t"
      "v_mfma_f32_16x16x32_bf16 v[220:223], v[196:199], v[144:147], v[220:223]\n\t"
      "ds_read_b128 v[196:199], v254 offset:8192\n\t"
      "v_mfma_f32_16x16x32_bf16 v[212:215], v[20:23], v[200:203], v[212:215]\n\t"
      "v_permlane32_swap_b32 v242, v243\n\t"
      "v_mfma_f32_16x16x32_bf16 v[216:219], v[84:87], v[200:203], v[216:219]\n\t"
      "v_max_f32 v252, v242, v243\n\t"
      "v_sub_f32 v250, v250, v252\n\t"
      "v_mfma_f32_16x16x32_bf16 v[220:223], v[200:203], v[148:151], v[220:223]\n\t"
      "ds_read_b128 v[200:203], v255 offset:9216\n\t"
      "v_sub_f32 v251, v251, v252\n\t"
      "s_waitcnt lgkmcnt(2)\n\t"
      "v_mfma_f32_16x16x32_bf16 v[212:215], v[24:27], v[204:207], v[212:215]\n\t"
      "v_mul_f32 v250, 0x3eb8aa3b, v250\n\t"
      "v_mul_f32 v251, 0x3eb8aa3b, v251\n\t"
      "v_mfma_f32_16x16x32_bf16 v[216:219], v[88:91], v[204:207], v[216:219]\n\t"
      "v_exp_f32 v250, v250\n\t"
      "v_mfma_f32_16x16x32_bf16 v[220:223], v[204:207], v[152:155], v[220:223]\n\t"
      "ds_read_b128 v[204:207], v254 offset:10240\n\t"
      "v_mfma_f32_16x16x32_bf16 v[212:215], v[28:31], v[208:211], v[212:215]\n\t"
      "v_exp_f32 v251, v251\n\t"
      "v_mfma_f32_16x16x32_bf16 v[216:219], v[92:95], v[208:211], v[216:219]\n\t"
      "v_add_f32 v252, v250, v251\n\t"
      "v_add_f32 v253, v250, v251\n\t"
      "v_mfma_f32_16x16x32_bf16 v[220:223], v[208:211], v[156:159], v[220:223]\n\t"
      "ds_read_b128 v[208:211], v255 offset:11264\n\t"
      "s_waitcnt lgkmcnt(2)\n\t"
      "v_mfma_f32_16x16x32_bf16 v[212:215], v[32:35], v[196:199], v[212:215]\n\t"
      "v_permlane16_swap_b32 v252, v253\n\t"
      "v_mfma_f32_16x16x32_bf16 v[216:219], v[96:99], v[196:199], v[216:219]\n\t"
      "v_add_f32 v242, v252, v253\n\t"
      "v_add_f32 v243, v252, v253\n\t"
      "v_mfma_f32_16x16x32_bf16 v[220:223], v[196:199], v[160:163], v[220:223]\n\t"
      "ds_read_b128 v[196:199], v254 offset:12288\n\t"
      "v_mfma_f32_16x16x32_bf16 v[212:215], v[36:39], v[200:203], v[212:215]\n\t"
      "v_permlane32_swap_b32 v242, v243\n\t"
      "v_mfma_f32_16x16x32_bf16 v[216:219], v[100:103], v[200:203], v[216:219]\n\t"
      "v_add_f32 v252, v242, v243\n\t"
      "v_add_u32 v253, %[so], v195\n\t"
      "v_mfma_f32_16x16x32_bf16 v[220:223], v[200:203], v[164:167], v[220:223]\n\t"
      "ds_read_b128 v[200:203], v255 offset:13312\n\t"
      "s_waitcnt lgkmcnt(2)\n\t"
      "v_mfma_f32_16x16x32_bf16 v[212:215], v[40:43], v[204:207], v[212:215]\n\t"
      "v_rcp_f32 v252, v252\n\t"
      "v_mfma_f32_16x16x32_bf16 v[216:219], v[104:107], v[204:207], v[216:219]\n\t"
      "v_mul_f32 v250, v250, v252\n\t"
      "v_mul_f32 v251, v251, v252\n\t"
      "v_mfma_f32_16x16x32_bf16 v[220:223], v[204:207], v[168:171], v[220:223]\n\t"
      "ds_read_b128 v[204:207], v254 offset:14336\n\t"
      "v_cvt_pk_bf16_f32 v250, v250, v250\n\t"
      "v_mfma_f32_16x16x32_bf16 v[212:215], v[44:47], v[208:211], v[212:215]\n\t"
      "v_cvt_pk_bf16_f32 v251, v251, v251\n\t"
      "v_and_b32 v250, v250, v194\n\t"
      "v_mfma_f32_16x16x32_bf16 v[216:219], v[108:111], v[208:211], v[216:219]\n\t"
      "v_and_b32 v251, v251, v194\n\t"
      "v_cndmask_b32 v246, 0, v250, %[c0]\n\t"
      "v_mfma_f32_16x16x32_bf16 v[220:223], v[208:211], v[172:175], v[220:223]\n\t"
      "ds_read_b128 v[208:211], v255 offset:15360\n\t"
      "v_cndmask_b32 v247, 0, v251, %[c1]\n\t"
      "s_waitcnt lgkmcnt(2)\n\t"
      "v_mfma_f32_16x16x32_bf16 v[212:215], v[48:51], v[196:199], v[212:215]\n\t"
      "v_cndmask_b32 v248, v250, 0, %[c0]\n\t"
      "v_cndmask_b32 v249, v251, 0, %[c1]\n\t"
      "v_mfma_f32_16x16x32_bf16 v[216:219], v[112:115], v[196:199], v[216:219]\n\t"
      "v_mfma_f32_16x16x32_bf16 v[220:223], v[196:199], v[176:179], v[220:223]\n\t"
      "ds_read_b128 v[196:199], v254 offset:16384\n\t"
      "v_mfma_f32_16x16x32_bf16 v[212:215], v[52:55], v[200:203], v[212:215]\n\t"
      "v_mfma_f32_16x16x32_bf16 v[238:241], v[230:233], v[246:249], 0\n\t"
      "v_mfma_f32_16x16x32_bf16 v[216:219], v[116:119], v[200:203], v[216:219]\n\t"
      "v_mfma_f32_16x16x32_bf16 v[220:223], v[200:203], v[180:183], v[220:223]\n\t"
      "ds_read_b128 v[200:203], v255 offset:17408\n\t"
      "s_waitcnt lgkmcnt(2)\n\t"
      "v_mfma_f32_16x16x32_bf16 v[212:215], v[56:59], v[204:207], v[212:215]\n\t"
      "v_mfma_f32_16x16x32_bf16 v[216:219], v[120:123], v[204:207], v[216:219]\n\t"
      "v_mfma_f32_16x16x32_bf16 v[220:223], v[204:207], v[184:187], v[220:223]\n\t"
      "ds_read_b128 v[204:207], v254 offset:18432\n\t"
      "v_cvt_pk_bf16_f32 v236, v238, v239\n\t"
      "v_mfma_f32_16x16x32_bf16 v[212:215], v[60:63], v[208:211], v[212:215]\n\t"
      "v_cvt_pk_bf16_f32 v237, v240, v241\n\t"
      "buffer_store_dwordx4 v[234:237], v253, %[rs], 0 offen sc1\n\t"
      "v_mfma_f32_16x16x32_bf16 v[216:219], v[124:127], v[208:211], v[216:219]\n\t"
      "v_mfma_f32_16x16x32_bf16 v[220:223], v[208:211], v[188:191], v[220:223]\n\t"
      "ds_read_b128 v[208:211], v255 offset:19456\n\t"
      "v_add_u32 v254, 0x4000, v254\n\t"
      "v_add_u32 v255, 0x4000, v255\n\t"
      "s_nop 0\n\t"
      "v_cvt_pk_bf16_f32 v224, v212, v213\n\t"
      "v_cvt_pk_bf16_f32 v225, v214, v215\n\t"
      "s_nop 0\n\t"
      "v_cvt_pk_bf16_f32 v228, v216, v217\n\t"
      "v_cvt_pk_bf16_f32 v229, v218, v219\n\t"
      "v_cvt_pk_bf16_f32 v232, v220, v221\n\t"
      "v_cvt_pk_bf16_f32 v233, v222, v223\n\t"
      "s_cmp_eq_u32 %[it], 0\n\t"
      "s_cselect_b32 %[bias], %[nb], 0\n\t"
      "s_cbranch_scc0 WCD%=\n\t"
      "v_mov_b32 v253, %[cnt]\n\t"
      "s_mov_b32 %[t0], 0x80000\n\t"
      "WC%=:\n\t"
      "ds_read_b32 v252, v253 offset:16\n\t"
      "s_waitcnt lgkmcnt(0)\n\t"
      "v_readfirstlane_b32 %[t1], v252\n\t"
      "s_cmp_ge_u32 %[t1], 4\n\t"
      "s_cbranch_scc1 WCD%=\n\t"
      "s_sleep 1\n\t"
      "s_sub_u32 %[t0], %[t0], 1\n\t"
      "s_cmp_lg_u32 %[t0], 0\n\t"
      "s_cbranch_scc1 WC%=\n\t"
      "WCD%=:\n\t"
      "s_waitcnt lgkmcnt(2)\n\t"
      "v_mfma_f32_16x16x32_bf16 v[212:215], v[0:3], v[196:199], 0\n\t"
      "v_mfma_f32_16x16x16_bf16 v[238:241], v[228:229], v[224:225], 0\n\t"
      "v_mfma_f32_16x16x32_bf16 v[216:219], v[64:67], v[196:199], 0\n\t"
      "v_mfma_f32_16x16x16_bf16 v[242:245], v[226:227], v[224:225], 0\n\t"
      "v_mfma_f32_16x16x32_bf16 v[220:223], v[196:199], v[128:131], 0\n\t"
      "ds_read_b128 v[196:199], v254 offset:4096\n\t"
      "v_mfma_f32_16x16x32_bf16 v[212:215], v[4:7], v[200:203], v[212:215]\n\t"
      "v_mfma_f32_16x16x32_bf16 v[216:219], v[68:71], v[200:203], v[216:219]\n\t"
      "v_mfma_f32_16x16x32_bf16 v[220:223], v[200:203], v[132:135], v[220:223]\n\t"
      "ds_read_b128 v[200:203], v255 offset:5120\n\t"
      "v_cndmask_b32 v250, v238, v239, %[par]\n\t"
      "s_waitcnt lgkmcnt(2)\n\t"
      "v_mfma_f32_16x16x32_bf16 v[212:215], v[8:11], v[204:207], v[212:215]\n\t"
      "v_cndmask_b32 v251, v240, v241, %[par]\n\t"
      "v_cndmask_b32 v242, v242, v243, %[par]\n\t"
      "v_mfma_f32_16x16x32_bf16 v[216:219], v[72:75], v[204:207], v[216:219]\n\t"
      "v_cndmask_b32 v244, v244, v245, %[par]\n\t"
      "v_add_f32 v242, %[bias], v242\n\t"
      "v_mfma_f32_16x16x32_bf16 v[220:223], v[204:207], v[136:139], v[220:223]\n\t"
      "ds_read_b128 v[204:207], v254 offset:6144\n\t"
      "v_add_f32 v244, %[bias], v244\n\t"
      "v_mfma_f32_16x16x32_bf16 v[212:215], v[12:15], v[208:211], v[212:215]\n\t"
      "v_cndmask_b32 v250, v242, v250, %[c0]\n\t"
      "v_cndmask_b32 v251, v244, v251, %[c1]\n\t"
      "v_mfma_f32_16x16x32_bf16 v[216:219], v[76:79], v[208:211], v[216:219]\n\t"
      "v_max_f32 v252, v250, v251\n\t"
      "v_max_f32 v253, v250, v251\n\t"
      "v_mfma_f32_16x16x32_bf16 v[220:223], v[208:211], v[140:143], v[220:223]\n\t"
      "ds_read_b128 v[208:211], v255 offset:7168\n\t"
      "s_waitcnt lgkmcnt(2)\n\t"
      "v_mfma_f32_16x16x32_bf16 v[212:215], v[16:19], v[196:199], v[212:215]\n\t"
      "v_permlane16_swap_b32 v252, v253\n\t"
      "v_mfma_f32_16x16x32_bf16 v[216:219], v[80:83], v[196:199], v[216:219]\n\t"
      "v_max_f32 v242, v252, v253\n\t"
      "v_max_f32 v243, v252, v253\n\t"
      "v_mfma_f32_16x16x32_bf16 v[220:223], v[196:199], v[144:147], v[220:223]\n\t"
      "ds_read_b128 v[196:199], v254 offset:8192\n\t"
      "v_mfma_f32_16x16x32_bf16 v[212:215], v[20:23], v[200:203], v[212:215]\n\t"
      "v_permlane32_swap_b32 v242, v243\n\t"
      "v_mfma_f32_16x16x32_bf16 v[216:219], v[84:87], v[200:203], v[216:219]\n\t"
      "v_max_f32 v252, v242, v243\n\t"
      "v_sub_f32 v250, v250, v252\n\t"
      "v_mfma_f32_16x16x32_bf16 v[220:223], v[200:203], v[148:151], v[220:223]\n\t"
      "ds_read_b128 v[200:203], v255 offset:9216\n\t"
      "v_sub_f32 v251, v251, v252\n\t"
      "s_waitcnt lgkmcnt(2)\n\t"
      "v_mfma_f32_16x16x32_bf16 v[212:215], v[24:27], v[204:207], v[212:215]\n\t"
      "v_mul_f32 v250, 0x3eb8aa3b, v250\n\t"
      "v_mul_f32 v251, 0x3eb8aa3b, v251\n\t"
      "v_mfma_f32_16x16x32_bf16 v[216:219], v[88:91], v[204:207], v[216:219]\n\t"
      "v_exp_f32 v250, v250\n\t"
      "v_mfma_f32_16x16x32_bf16 v[220:223], v[204:207], v[152:155], v[220:223]\n\t"
      "ds_read_b128 v[204:207], v254 offset:10240\n\t"
      "v_mfma_f32_16x16x32_bf16 v[212:215], v[28:31], v[208:211], v[212:215]\n\t"
      "v_exp_f32 v251, v251\n\t"
      "v_mfma_f32_16x16x32_bf16 v[216:219], v[92:95], v[208:211], v[216:219]\n\t"
      "v_add_f32 v252, v250, v251\n\t"
      "v_add_f32 v253, v250, v251\n\t"
      "v_mfma_f32_16x16x32_bf16 v[220:223], v[208:211], v[156:159], v[220:223]\n\t"
      "ds_read_b128 v[208:211], v255 offset:11264\n\t"
      "s_waitcnt lgkmcnt(2)\n\t"
      "v_mfma_f32_16x16x32_bf16 v[212:215], v[32:35], v[196:199], v[212:215]\n\t"
      "v_permlane16_swap_b32 v252, v253\n\t"
      "v_mfma_f32_16x16x32_bf16 v[216:219], v[96:99], v[196:199], v[216:219]\n\t"
      "v_add_f32 v242, v252, v253\n\t"
      "v_add_f32 v243, v252, v253\n\t"
      "v_mfma_f32_16x16x32_bf16 v[220:223], v[196:199], v[160:163], v[220:223]\n\t"
      "ds_read_b128 v[196:199], v254 offset:12288\n\t"
      "v_mfma_f32_16x16x32_bf16 v[212:215], v[36:39], v[200:203], v[212:215]\n\t"
      "v_permlane32_swap_b32 v242, v243\n\t"
      "v_mfma_f32_16x16x32_bf16 v[216:219], v[100:103], v[200:203], v[216:219]\n\t"
      "v_add_f32 v252, v242, v243\n\t"
      "v_mfma_f32_16x16x32_bf16 v[220:223], v[200:203], v[164:167], v[220:223]\n\t"
      "ds_read_b128 v[200:203], v255 offset:13312\n\t"
      "s_waitcnt lgkmcnt(2)\n\t"
      "v_mfma_f32_16x16x32_bf16 v[212:215], v[40:43], v[204:207], v[212:215]\n\t"
      "v_rcp_f32 v252, v252\n\t"
      "v_mfma_f32_16x16x32_bf16 v[216:219], v[104:107], v[204:207], v[216:219]\n\t"
      "v_mul_f32 v250, v250, v252\n\t"
      "v_mul_f32 v251, v251, v252\n\t"
      "v_mfma_f32_16x16x32_bf16 v[220:223], v[204:207], v[168:171], v[220:223]\n\t"
      "ds_read_b128 v[204:207], v254 offset:14336\n\t"
      "v_cvt_pk_bf16_f32 v250, v250, v250\n\t"
      "v_mfma_f32_16x16x32_bf16 v[212:215], v[44:47], v[208:211], v[212:215]\n\t"
      "v_cvt_pk_bf16_f32 v251, v251, v251\n\t"
      "v_and_b32 v250, v250, v194\n\t"
      "v_mfma_f32_16x16x32_bf16 v[216:219], v[108:111], v[208:211], v[216:219]\n\t"
      "v_and_b32 v251, v251, v194\n\t"
      "v_cndmask_b32 v248, 0, v250, %[c0]\n\t"
      "v_mfma_f32_16x16x32_bf16 v[220:223], v[208:211], v[172:175], v[220:223]\n\t"
      "ds_read_b128 v[208:211], v255 offset:15360\n\t"
      "v_cndmask_b32 v249, 0, v251, %[c1]\n\t"
      "s_waitcnt lgkmcnt(2)\n\t"
      "v_mfma_f32_16x16x32_bf16 v[212:215], v[48:51], v[196:199], v[212:215]\n\t"
      "v_cndmask_b32 v246, v250, 0, %[c0]\n\t"
      "v_cndmask_b32 v247, v251, 0, %[c1]\n\t"
      "v_mfma_f32_16x16x32_bf16 v[216:219], v[112:115], v[196:199], v[216:219]\n\t"
      "v_mfma_f32_16x16x32_bf16 v[220:223], v[196:199], v[176:179], v[220:223]\n\t"
      "ds_read_b128 v[196:199], v254 offset:16384\n\t"
      "v_mfma_f32_16x16x32_bf16 v[212:215], v[52:55], v[200:203], v[212:215]\n\t"
      "v_mfma_f32_16x16x32_bf16 v[238:241], v[230:233], v[246:249], 0\n\t"
      "v_mfma_f32_16x16x32_bf16 v[216:219], v[116:119], v[200:203], v[216:219]\n\t"
      "v_mfma_f32_16x16x32_bf16 v[220:223], v[200:203], v[180:183], v[220:223]\n\t"
      "ds_read_b128 v[200:203], v255 offset:17408\n\t"
      "s_waitcnt lgkmcnt(2)\n\t"
      "v_mfma_f32_16x16x32_bf16 v[212:215], v[56:59], v[204:207], v[212:215]\n\t"
      "v_mfma_f32_16x16x32_bf16 v[216:219], v[120:123], v[204:207], v[216:219]\n\t"
      "v_mfma_f32_16x16x32_bf16 v[220:223], v[204:207], v[184:187], v[220:223]\n\t"
      "ds_read_b128 v[204:207], v254 offset:18432\n\t"
      "v_cvt_pk_bf16_f32 v234, v238, v239\n\t"
      "v_mfma_f32_16x16x32_bf16 v[212:215], v[60:63], v[208:211], v[212:215]\n\t"
      "v_cvt_pk_bf16_f32 v235, v240, v241\n\t"
      "v_mfma_f32_16x16x32_bf16 v[216:219], v[124:127], v[208:211], v[216:219]\n\t"
      "v_mfma_f32_16x16x32_bf16 v[220:223], v[208:211], v[188:191], v[220:223]\n\t"
      "ds_read_b128 v[208:211], v255 offset:19456\n\t"
      "v_add_u32 v254, 0x4000, v254\n\t"
      "v_add_u32 v255, 0x4000, v255\n\t"
      "s_nop 1\n\t"
      "v_cvt_pk_bf16_f32 v224, v212, v213\n\t"
      "v_cvt_pk_bf16_f32 v225, v214, v215\n\t"
      "v_cvt_pk_bf16_f32 v226, v216, v217\n\t"
      "v_cvt_pk_bf16_f32 v227, v218, v219\n\t"
      "v_cvt_pk_bf16_f32 v230, v220, v221\n\t"
      "v_cvt_pk_bf16_f32 v231, v222, v223\n\t"
      "s_mov_b32 %[bias], 0\n\t"
      "s_mov_b32 %[so], %[ob]\n\t"
      "s_mov_b64 %[fmask], 1\n\t"
      "s_add_u32 %[it], %[it], 1\n\t"
      "s_cmp_lt_u32 %[it], 2\n\t"
      "s_cbranch_scc1 LOOP%=\n\t"
      "s_waitcnt lgkmcnt(0)\n\t"
            : [it] "=&s"(it), [so] "=&s"(so), [bias] "=&s"(bias), [t0] "=&s"(t0), [t1] "=&s"(t1), [fmask] "=&s"(fmask), [par] "=&s"(par), [c0] "=&s"(c0), [c1] "=&s"(c1),
              "=&{v[224:225]}"(qc), "=&{v[226:227]}"(k0), "=&{v[228:229]}"(k1), "=&{v[230:233]}"(vv), "=&{v[234:237]}"(od), "=&{v[194:195]}"(misc)
            : WOPS(W), [rs] "s"(rwsv), [tag] "s"(tag), [lds] "s"(lbase), [cnt] "s"(cb + 4 * C_H1), [ob] "s"(obase), [fl] "s"(flag_off(0, bl1 * 8 + hg1, w)),
              [nb] "s"(__builtin_amdgcn_readfirstlane(bl1 == 0 ? 0xf149f2cau : 0u))
            : "memory", "scc", CLOB_P1);
      }
      sig<C_P1DONE>(cb);
      asm volatile(
      "s_nop 1\n\t"
      "v_mfma_f32_16x16x16_bf16 v[238:241], v[226:227], v[224:225], 0\n\t"
      "v_mfma_f32_16x16x16_bf16 v[242:245], v[228:229], v[224:225], 0\n\t"
      "s_nop 6\n\t"
      "v_cndmask_b32 v250, v238, v239, %[par]\n\t"
      "v_cndmask_b32 v251, v240, v241, %[par]\n\t"
      "v_cndmask_b32 v242, v242, v243, %[par]\n\t"
      "v_cndmask_b32 v244, v244, v245, %[par]\n\t"
      "v_add_f32 v242, %[bias], v242\n\t"
      "v_add_f32 v244, %[bias], v244\n\t"
      "v_cndmask_b32 v250, v242, v250, %[c0]\n\t"
      "v_cndmask_b32 v251, v244, v251, %[c1]\n\t"
      "v_max_f32 v252, v250, v251\n\t"
      "v_max_f32 v253, v250, v251\n\t"
      "s_nop 1\n\t"
      "v_permlane16_swap_b32 v252, v253\n\t"
      "v_max_f32 v242, v252, v253\n\t"
      "v_max_f32 v243, v252, v253\n\t"
      "s_nop 1\n\t"
      "v_permlane32_swap_b32 v242, v243\n\t"
      "v_max_f32 v252, v242, v243\n\t"
      "v_sub_f32 v250, v250, v252\n\t"
      "v_sub_f32 v251, v251, v252\n\t"
      "v_mul_f32 v250, 0x3eb8aa3b, v250\n\t"
      "v_mul_f32 v251, 0x3eb8aa3b, v251\n\t"
      "v_exp_f32 v250, v250\n\t"
      "v_exp_f32 v251, v251\n\t"
      "s_nop 0\n\t"
      "v_add_f32 v252, v250, v251\n\t"
      "v_add_f32 v253, v250, v251\n\t"
      "s_nop 1\n\t"
      "v_permlane16_swap_b32 v252, v253\n\t"
      "v_add_f32 v242, v252, v253\n\t"
      "v_add_f32 v243, v252, v253\n\t"
      "s_nop 1\n\t"
      "v_permlane32_swap_b32 v242, v243\n\t"
      "v_add_f32 v252, v242, v243\n\t"
      "v_rcp_f32 v252, v252\n\t"
      "s_nop 0\n\t"
      "v_mul_f32 v250, v250, v252\n\t"
      "v_mul_f32 v251, v251, v252\n\t"
      "v_cvt_pk_bf16_f32 v250, v250, v250\n\t"
      "v_cvt_pk_bf16_f32 v251, v251, v251\n\t"
      "v_and_b32 v250, v250, v194\n\t"
      "v_and_b32 v251, v251, v194\n\t"
      "v_cndmask_b32 v246, 0, v250, %[c0]\n\t"
      "v_cndmask_b32 v247, 0, v251, %[c1]\n\t"
      "v_cndmask_b32 v248, v250, 0, %[c0]\n\t"
      "v_cndmask_b32 v249, v251, 0, %[c1]\n\t"
      "s_nop 1\n\t"
      "v_mfma_f32_16x16x32_bf16 v[238:241], v[230:233], v[246:249], 0\n\t"
      "v_add_u32 v253, %[ob], v195\n\t"
      "s_nop 6\n\t"
      "v_cvt_pk_bf16_f32 v236, v238, v239\n\t"
      "v_cvt_pk_bf16_f32 v237, v240, v241\n\t"
      "buffer_store_dwordx4 v[234:237], v253, %[rs], 0 offen sc1\n\t"
      "s_nop 1\n\t"
          : "+{v[234:237]}"(od)
          : "{v[224:225]}"(qc), "{v[226:227]}"(k0), "{v[228:229]}"(k1), "{v[230:233]}"(vv), "{v[194:195]}"(misc), [par] "s"(par), [c0] "s"(c0), [c1] "s"(c1),
            [rs] "s"(rwsv), [ob] "s"(obase + 32 * 1024), [bias] "s"(0u)
          : "memory", "v238", "v239", "v240", "v241", "v242", "v243", "v244", "v245", "v246", "v247", "v248", "v249", "v250", "v251", "v252", "v253");
    }
    RELANE();
    const int n0 = 64 * fs;
    const int fcol = n0 + 16 * q + 8 * (w & 1);
    auto red_put = [&](const f32x4& a, const f32x4& b, int pl, u32 scr) {
      const u32 dst = scr + ((w ^ pl) * 3 + pl - 1) * 2048 + fresh_lane() * 16;
      *(LAS f32x4*)(L + dst) = a; *(LAS f32x4*)(L + dst + 1024) = b;
    };
    auto red_get = [&](f32x4& a, f32x4& b, u32 scr) {
      const u32 src = scr + w * 6144 + fresh_lane() * 16;
#pragma unroll
      for (int pl = 0; pl < 3; ++pl) { a += *(LAS f32x4*)(L + src + pl * 2048); b += *(LAS f32x4*)(L + src + pl * 2048 + 1024); }
    };
    v4u W2[11][4];
    const v4u rw2 = mk_rsrc((const unsigned char*)w2 - 4096, F * D * 2 + 4096);
    auto dma_x4 = [&](v4u rs, const u32 (&vo)[4], const u32 (&so)[4], u32 dst) {
      asm volatile("s_mov_b32 m0, %9\n\ts_nop 0\n\tbuffer_load_dwordx4 %0, %4, %5 offen lds\n\tbuffer_load_dwordx4 %1, %4, %6 offen offset:1024 lds\n\t"
                   "buffer_load_dwordx4 %2, %4, %7 offen offset:2048 lds\n\tbuffer_load_dwordx4 %3, %4, %8 offen offset:3072 lds"
                   ::"v"(vo[0]), "v"(vo[1]), "v"(vo[2]), "v"(vo[3]), "s"(rs), "s"(so[0]), "s"(so[1]), "s"(so[2]), "s"(so[3]), "s"(dst) : "memory");
    };
    struct Ring { u32 Q[3], cpart, tb[2]; int jj; };
    auto ring_setup = [&]() -> Ring {
      RELANE();
      Ring R;
      R.jj = lane >> 3;
      R.cpart = (u32)(n0 * 2 + (((lane & 7) ^ ((R.jj >> 1) & 1)) * 16));
      const int c0 = 44 * w - 3 * ((44 * w) / 3);
#pragma unroll
      for (int j = 0; j < 3; ++j) {
        const int gs = c0 + j, g = gs >= 3 ? gs - 3 : gs;
        const int ra = 4 * (g & 1) + 2 * (g >> 1), rb = 8 * g + 6 - 24;
        R.Q[j] = (u32)((R.jj < 6 ? ra : rb) + 8 * (R.jj >> 1) + (R.jj & 1) + (gs >= 3 ? 24 : 0)) * 1024u + R.cpart;
      }
      const int i4 = (lane & 15) >> 2, pp = lane & 3;
#pragma unroll
      for (int h = 0; h < 2; ++h) R.tb[h] = w * L_TILE + (8 * q + i4) * 128 + (((2 * pp + (h ^ (w & 1))) ^ ((i4 >> 1) & 1)) * 16);
      return R;
    };
    auto w2_issue = [&](const Ring& R, int kk, u32 base) {
      const int n = 4 * kk;
      const u32 sb = (u32)((44 * w) / 3) * 24576u + 4096u;
      u32 vo[4], so[4];
#pragma unroll
      for (int qq = 0; qq < 4; ++qq) {
        vo[qq] = R.Q[(n + qq) % 3]; so[qq] = sb + ((n + qq) / 3) * 24576u - qq * 1024u;
        if (kk >= 9) { vo[qq] = __builtin_elementwise_min(vo[qq] + so[qq], (u32)(F * D * 2 + 4080 - qq * 1024)); so[qq] = 0u; }
      }
      dma_x4(rw2, vo, so, lbase + base + w * L_TILE + (kk & 3) * 4096);
    };
    auto ring_read = [&](const Ring& R, v4u (&o)[4], u32 off) {
#pragma unroll
      for (int t = 0; t < 4; ++t) {
        const v2u a = lds_tr(L, R.tb[t >> 1] + off + (t & 1) * 8), b = lds_tr(L, R.tb[t >> 1] + off + (t & 1) * 8 + 512);
        o[t] = v4u{a.x, a.y, b.x, b.y};
      }
#pragma unroll
      for (int t = 0; t < 4; ++t) PIN(o[t]);
    };
    {
      v4u Wo[4][4], xres[2];
      const u32 pfo = flag_off(1, bl * 8 + 2 * w + ((lane >> 2) & 1), lane & 3);
      u32 pf;
      {
        const Ring R = ring_setup();
        const v4u rwo = mk_rsrc(wo, D * D * 2);
        const u32 vw = (u32)((R.jj >> 2) * 16 + (R.jj & 3)) * 1024u + R.cpart;
        __builtin_amdgcn_s_setprio(0);
        DRAIN(); set_flag(rwsv, flag_off(1, bl1 * 8 + hg1, w), tag);
        cwait<C_P1DONE>(cb, 4u);
#pragma unroll
        for (int i = 0; i < 4; ++i) {
          const u32 sb = (u32)(4 * w + ((i + rot) & 3)) * 32768u;
          const u32 vo[4] = {vw, vw, vw, vw}, so[4] = {sb, sb + 3072u, sb + 6144u, sb + 9216u};
          dma_x4(rwo, vo, so, lbase + w * L_TILE + i * 4096);
        }
        vmwait<12>(); ring_read(R, Wo[0], 0u);
        vmwait<8>(); ring_read(R, Wo[1], 4096u);
        vmwait<4>(); ring_read(R, Wo[2], 8192u);
        vmwait<0>();
        pf = ld_flag(rws, pfo);
#pragma unroll
        for (int st = 0; st < 2; ++st) xres[st] = *(const GAS v4u*)(x + (size_t)((2 * (2 * bl + (w >> 1)) + st) * 16 + tok) * D + fcol);
        ring_read(R, Wo[3], 12288u);
        sig<C_YBAR>(cb);
      }
      if (!wave_all(pf == tag)) poll_flags(rws, pfo, tag);
      __builtin_amdgcn_fence(__ATOMIC_ACQUIRE, "wavefront"); CBAR();
      v4u o[2][8];
#pragma unroll
      for (int st = 1; st >= 0; --st)
#pragma unroll
        for (int h = 0; h < 8; ++h)
          o[st][h] = __builtin_amdgcn_raw_buffer_load_b128(rws, lane * 16 + (h & 1) * 1024, WS_O + ((2 * bl + (st ^ (w >> 1))) * 32 + 8 * w + 2 * (((h >> 1) + rot) & 3)) * 1024, AUX_SC1);
      SB0();
      cwait<C_YBAR>(cb, 8u);
      f32x4 acc[2][4];
#pragma unroll
      for (int st = 1; st >= 0; --st) {
#pragma unroll
        for (int tt = 0; tt < 2; ++tt)
#pragma unroll
          for (int t = 0; t < 4; ++t) acc[tt][t] = f32x4{0.f, 0.f, 0.f, 0.f};
#pragma unroll
        for (int i = 0; i < 4; ++i)
#pragma unroll
          for (int tt = 0; tt < 2; ++tt) {
            const v4u f = {o[st][2 * i][2 * tt], o[st][2 * i][2 * tt + 1], o[st][2 * i + 1][2 * tt], o[st][2 * i + 1][2 * tt + 1]};
#pragma unroll
            for (int t = 0; t < 4; ++t) acc[tt][t] = mfma32(Wo[i][t], f, acc[tt][t]);
          }
#pragma unroll
        for (int tt = 0; tt < 2; ++tt) {
          if (st) red_put(acc[tt][0], acc[tt][1], 2, L_SCR2 + tt * 24576);
          red_put(acc[tt][2], acc[tt][3], 2 * st + 1, L_SCR2 + tt * 24576);
        }
        SB0();
      }
      sig<C_RED>(cb);
      cwait<C_MOD2>(cb, 1u);
      const f32x4 g0 = *(LAS f32x4*)(L + L_GA + (fcol - n0) * 4), g1 = *(LAS f32x4*)(L + L_GA + (fcol - n0) * 4 + 16);
      const v4u a2 = lds_r128(L, L_AS2 + fcol * 2);
      cwait<C_RED, 0>(cb, 4u);
#pragma unroll
      for (int st = 0; st < 2; ++st) {
        const int mytile = 2 * (2 * bl + (w >> 1)) + st;
        red_get(acc[st][0], acc[st][1], L_SCR2 + st * 24576);
        const float res[8] = {acc[st][0][0], acc[st][0][1], acc[st][0][2], acc[st][0][3], acc[st][1][0], acc[st][1][1], acc[st][1][2], acc[st][1][3]};
        float x1[8];
#pragma unroll
        for (int d = 0; d < 4; ++d) {
          x1[2 * d] = lo16(xres[st][d]) + (d < 2 ? g0[2 * d] : g1[2 * d - 4]) * res[2 * d];
          x1[2 * d + 1] = hi16(xres[st][d]) + (d < 2 ? g0[2 * d + 1] : g1[2 * d - 3]) * res[2 * d + 1];
        }
        v4u y, xb; float sm = 0.f, sq = 0.f;
#pragma unroll
        for (int d = 0; d < 4; ++d) {
          y[d] = pk2(x1[2 * d] * lo16(a2[d]), x1[2 * d + 1] * hi16(a2[d]));
          xb[d] = pk2(x1[2 * d], x1[2 * d + 1]);
          sm += x1[2 * d] + x1[2 * d + 1]; sq += x1[2 * d] * x1[2 * d] + x1[2 * d + 1] * x1[2 * d + 1];
        }
        __builtin_amdgcn_raw_buffer_store_b128(y, rws, WS_X1 + (mytile * 16 + 2 * fs + (w & 1)) * 1024 + lane * 16, 0, AUX_SC1);
        sm = x32_add(x16_add(sm)); sq = x32_add(x16_add(sq));
        __builtin_amdgcn_raw_buffer_store_b64(v2u{fu(sm), fu(sq)}, rws, q == 0 ? WS_ST + (mytile * 16 + 2 * fs + (w & 1)) * 128 + tok * 8 : OOB, 0, AUX_SC1);
        *(GAS v4u*)(out + (size_t)(mytile * 16 + tok) * D + fcol) = xb;
      }
      DRAIN(); set_flag(rwsv, flag_off(2, wg, w), tag);
      sig<C_REDD>(cb);
    }
    __builtin_amdgcn_s_setprio(1);
    {
      const Ring R = ring_setup();
      cwait<C_REDD>(cb, 4u);
#pragma unroll
      for (int part = 0; part < 2; ++part) {
        const int k0 = part ? 6 : 0, k1 = part ? 11 : 6;
        const u32 base = part ? 65536u : 0u;
        if (part && r < 15) {
          cwait<C_H2, 2>(cb, 4u);
          stage(1, tag);
          sig<C_H2 + 1>(cb);
          cwait<C_H2 + 2, 4>(cb, 4u);
        }
        if (part && r == 15) cwait<C_PART>(cb, 4u);
#pragma unroll
        for (int kk = k0; kk < k0 + 4; ++kk) w2_issue(R, kk, base);
#pragma unroll
        for (int kk = k0; kk < k1; ++kk) {
          const int after = (kk + 3 < k1 ? 3 : k1 - 1 - kk);
          if (after == 3) vmwait<12>(); else if (after == 2) vmwait<8>(); else if (after == 1) vmwait<4>(); else vmwait<0>();
          ring_read(R, W2[kk], base + (kk & 3) * 4096);
          if (kk + 4 < k1) w2_issue(R, kk + 4, base);
        }
      }
    }
    {
#pragma unroll 1
      for (int st = 0; st < 2; ++st) {
        const int ul = 2 * (r >> 3) + st, u = 4 * cl + ul;
        const int lane4 = fresh_lane();
        int vpoll = (44 * w) / 3 + (lane4 & 15); vpoll = vpoll > (44 * w + 43) / 3 ? (44 * w + 43) / 3 : vpoll;
        if (ul == 3 && r < 15) cwait<27, 2>(cb, 4u);
        poll_flags(rws, flag_off(4 + ul, cl * 16 + (vpoll >> 2), vpoll & 3), tag);
        const u32 hv0 = WS_HM + (2 * u + 1 - (w >> 1)) * HM_TILE + 11 * w * 1024 + lane4 * 16, hv1 = WS_HM + (2 * u + (w >> 1)) * HM_TILE + 11 * w * 1024 + lane4 * 16;
        constexpr int RING = 11;
        v4u hf[RING];
        auto hload = [&](int i) -> v4u { return __builtin_amdgcn_raw_buffer_load_b128(rws, (i < 11 ? hv0 : hv1) + ((i % 11) & 3) * 1024, ((i % 11) >> 2) * 4096, AUX_SC1); };
#pragma unroll
        for (int i = 0; i < RING; ++i) hf[i] = hload(i);
        SB0();
        cwait<C_REDD>(cb, 4u * (st + 1));
        if (st == 0) { if (r < 15) { cwait<C_H2>(cb, 4u); cwait<C_H2 + 1>(cb, 4u); } }
        f32x4 acc[4];
#pragma unroll
        for (int i = 0; i < 22; ++i) {
          if (i % 11 == 0) {
#pragma unroll
            for (int t = 0; t < 4; ++t) acc[t] = f32x4{0.f, 0.f, 0.f, 0.f};
          }
#pragma unroll
          for (int t = 0; t < 4; ++t) acc[t] = mfma32(W2[i % 11][t], hf[i % RING], acc[t]);
          SB0();
          if (i + RING < 22) hf[i % RING] = hload(i + RING);
          if (i == 10) { red_put(acc[0], acc[1], 2, L_SCR4); red_put(acc[2], acc[3], 3, L_SCR4); }
          SB0();
        }
        red_put(acc[2], acc[3], 1, L_SCR4);
        sig<C_RED>(cb);
        RELANE();
        const int fcol = n0 + 16 * q + 8 * (w & 1);
        u16* op = out + (size_t)((2 * u + (w >> 1)) * 16 + tok) * D + fcol;
        const v4u xb = *(const GAS v4u*)op;
        cwait<C_RED, 0>(cb, 4u * (st + 2));
        red_get(acc[0], acc[1], L_SCR4);
        sig<C_REDD>(cb);
        const float res[8] = {acc[0][0], acc[0][1], acc[0][2], acc[0][3], acc[1][0], acc[1][1], acc[1][2], acc[1][3]};
        const f32x4 g0 = *(LAS f32x4*)(L + L_GB + (fcol - n0) * 4), g1 = *(LAS f32x4*)(L + L_GB + (fcol - n0) * 4 + 16);
        v4u y;
        y.x = pk2(lo16(xb.x) + g0[0] * res[0], hi16(xb.x) + g0[1] * res[1]);
        y.y = pk2(lo16(xb.y) + g0[2] * res[2], hi16(xb.y) + g0[3] * res[3]);
        y.z = pk2(lo16(xb.z) + g1[0] * res[4], hi16(xb.z) + g1[1] * res[5]);
        y.w = pk2(lo16(xb.w) + g1[2] * res[6], hi16(xb.w) + g1[3] * res[7]);
        *(GAS v4u*)op = y;
      }
    }
  }
}
}
#if defined(__HIP_DEVICE_COMPILE__)
#pragma clang attribute pop
#endif

extern "C" void kernel_launch(void* const* d_in, const int* in_sizes, int n_in, void* d_out, int out_size, void* d_ws, size_t ws_size,
                              hipStream_t stream) {
  using mk::u16;
  mk::mega<<<256, 512, 0, stream>>>((const u16*)d_in[0], (const u16*)d_in[1], (const u16*)d_in[2], (const u16*)d_in[4], (const u16*)d_in[5],
                                    (const u16*)d_in[6], (const u16*)d_in[7], (const u16*)d_in[8], (u16*)d_out, (unsigned char*)d_ws);
}
```
